```python
import torch

def _softmax(x, axis=-1):
    return torch.softmax(x, dim=axis)
def _gelu(x):
    return torch.nn.functional.gelu(x, approximate="tanh")
def _silu(x):
    return torch.nn.functional.silu(x)
def _sigmoid(x):
    return torch.sigmoid(x)
def _layernorm(x, axis=-1, eps=1e-5):
    m = x.mean(dim=axis, keepdim=True)
    v = x.var(dim=axis, keepdim=True, unbiased=False)
    return (x - m) / torch.sqrt(v + eps)
def _scatter_add(tgt, idx, src, axis=0):
    return tgt.clone().index_add_(axis, idx, src)
def _segment_sum(src, seg, n):
    out = torch.zeros((n,) + src.shape[1:], dtype=src.dtype)
    return out.index_add_(0, seg, src)
def _pad(x, pw):
    flat = tuple(v for pair in reversed(pw) for v in pair)
    return torch.nn.functional.pad(x, flat)

kernel_name = 'standalone__mha_block_cached__sampling'

def setup_inputs():

    _w = (lambda a: (lambda t: t.to(torch.bfloat16) if t.is_floating_point() else t)(torch.as_tensor(a)))
    B, Sq, D = 16, 1, 1024
    H, Sk, Dh = 32, 2048, 128
    return {
        "x": _w((lambda *s: torch.randn(*s, dtype=torch.bfloat16) * 0.8)(B, Sq, D)),
        "k_cache": _w((lambda *s: torch.randn(*s, dtype=torch.bfloat16) * 0.8)(B, H, Sk, Dh)),
        "v_cache": _w((lambda *s: torch.randn(*s, dtype=torch.bfloat16) * 0.05)(B, H, Sk, Dh)),
        "wq": _w((lambda *s: torch.randn(*s, dtype=torch.bfloat16) * 0.8)(D, H * Dh) * (D ** -0.5)),
        "wo": _w((lambda *s: torch.randn(*s, dtype=torch.bfloat16) * 0.05)(H * Dh, D) * ((H * Dh) ** -0.5)),
    }

def reference(x, k_cache, v_cache, wq, wo):
    B, Sq, D = x.shape
    H, Dh = 32, 128
    q = (lambda x, *d: x.permute(*d))((x @ wq).reshape(B, Sq, H, Dh), 0, 2, 1, 3)
    scores = torch.einsum("bhqd,bhkd->bhqk", q, k_cache) * (Dh ** -0.5)
    p = _softmax(scores, axis=-1)
    o = torch.einsum("bhqk,bhkd->bhqd", p, v_cache)
    return (lambda x, *d: x.permute(*d))(o, 0, 2, 1, 3).reshape(B, Sq, H * Dh) @ wo
```

```cpp
#include <hip/hip_runtime.h>
#include <hip/hip_bf16.h>
#include <stdint.h>

typedef unsigned u32x4 __attribute__((ext_vector_type(4)));
typedef float f32x4 __attribute__((ext_vector_type(4)));
typedef __bf16 bf16x2 __attribute__((ext_vector_type(2)));

constexpr int B = 16, D = 1024, H = 32, SK = 2048, DH = 128, HD = H * DH;

__device__ __forceinline__ float dot2(unsigned a, unsigned b, float acc) {
  return __builtin_amdgcn_fdot2_f32_bf16(__builtin_bit_cast(bf16x2, a), __builtin_bit_cast(bf16x2, b), acc, false);
}
__device__ __forceinline__ float bflo(unsigned u) { return __builtin_bit_cast(float, u << 16); }
__device__ __forceinline__ float bfhi(unsigned u) { return __builtin_bit_cast(float, u & 0xffff0000u); }
__device__ __forceinline__ unsigned short f2bf(float f) { __hip_bfloat16 h = __float2bfloat16(f); return __builtin_bit_cast(unsigned short, h); }
template <int CTRL> __device__ __forceinline__ float dpp(float x) {
  return __builtin_bit_cast(float, __builtin_amdgcn_mov_dpp(__builtin_bit_cast(int, x), CTRL, 0xf, 0xf, true));
}
constexpr int XOR1 = 0xB1, XOR2 = 0x4E, XOR7 = 0x141, XOR8 = 0x128;

template <int K, int N, bool OUT_BF16>
__global__ void __launch_bounds__(256) gemm16(const unsigned short* __restrict__ A, const unsigned short* __restrict__ W, unsigned short* __restrict__ out) {
  __shared__ float red[128][16][17];
  const int tid = threadIdx.x, kq = tid >> 1, half = tid & 1;
  const int c0 = blockIdx.x * 16 + half * 8;
  float acc[16][8];
#pragma unroll
  for (int b = 0; b < 16; ++b)
#pragma unroll
    for (int j = 0; j < 8; ++j) acc[b][j] = 0.f;
  for (int k = kq; k < K; k += 128) {
    u32x4 w = *(const u32x4*)(W + (size_t)k * N + c0);
    float wf[8];
#pragma unroll
    for (int j = 0; j < 4; ++j) { wf[2 * j] = bflo(w[j]); wf[2 * j + 1] = bfhi(w[j]); }
#pragma unroll
    for (int b = 0; b < 16; ++b) {
      float a = bflo((unsigned)A[b * K + k]);
#pragma unroll
      for (int j = 0; j < 8; ++j) acc[b][j] = fmaf(a, wf[j], acc[b][j]);
    }
  }
#pragma unroll
  for (int b = 0; b < 16; ++b)
#pragma unroll
    for (int j = 0; j < 8; ++j) red[kq][b][half * 8 + j] = acc[b][j];
  __syncthreads();
  {
    const int b = tid >> 4, c = tid & 15;
    float s = 0.f;
    for (int i = 0; i < 128; ++i) s += red[i][b][c];
    out[b * N + blockIdx.x * 16 + c] = f2bf(s);
  }
}

__global__ void __launch_bounds__(256) attn(const unsigned short* __restrict__ q, const unsigned short* __restrict__ kc, const unsigned short* __restrict__ vc, unsigned short* __restrict__ o) {
  __shared__ float mrg[4][132];
  const int bh = blockIdx.x;
  const int b = bh / H, h = bh % H;
  const int tid = threadIdx.x, lane = tid & 63, wave = tid >> 6, r = lane >> 4, c = lane & 15;
  const u32x4 qv = *(const u32x4*)(q + b * HD + h * DH + 8 * c);
  const u32x4* kp = (const u32x4*)(kc + (size_t)bh * SK * DH) + lane;
  const u32x4* vp = (const u32x4*)(vc + (size_t)bh * SK * DH) + lane;
  constexpr float C2 = 0.088388347648318440f * 1.4426950408889634f;
  float m = -INFINITY, l = 0.f, acc[8];
#pragma unroll
  for (int j = 0; j < 8; ++j) acc[j] = 0.f;
  constexpr int NT = SK / 32 / 4;
  u32x4 Kn[8], Vn[8];
#pragma unroll
  for (int i = 0; i < 8; ++i) { Kn[i] = __builtin_nontemporal_load(kp + (wave * 8 + i) * 64); }
#pragma unroll
  for (int i = 0; i < 8; ++i) { Vn[i] = __builtin_nontemporal_load(vp + (wave * 8 + i) * 64); }
  for (int t = 0; t < NT; ++t) {
    u32x4 Kc[8], Vc[8];
#pragma unroll
    for (int i = 0; i < 8; ++i) { Kc[i] = Kn[i]; Vc[i] = Vn[i]; }
    if (t + 1 < NT) {
#pragma unroll
      for (int i = 0; i < 8; ++i) { Kn[i] = __builtin_nontemporal_load(kp + (((t + 1) * 4 + wave) * 8 + i) * 64); }
#pragma unroll
      for (int i = 0; i < 8; ++i) { Vn[i] = __builtin_nontemporal_load(vp + (((t + 1) * 4 + wave) * 8 + i) * 64); }
    }
    float s[8];
#pragma unroll
    for (int i = 0; i < 8; ++i) {
      float d = 0.f;
#pragma unroll
      for (int j = 0; j < 4; ++j) d = dot2(Kc[i][j], qv[j], d);
      d += dpp<XOR1>(d); d += dpp<XOR2>(d); d += dpp<XOR7>(d); d += dpp<XOR8>(d);
      s[i] = d * C2;
    }
    float pm = s[0];
#pragma unroll
    for (int i = 1; i < 8; ++i) pm = fmaxf(pm, s[i]);
    const float mn = fmaxf(m, pm);
    const float alpha = __builtin_amdgcn_exp2f(m - mn);
    m = mn;
    l *= alpha;
#pragma unroll
    for (int j = 0; j < 8; ++j) acc[j] *= alpha;
#pragma unroll
    for (int i = 0; i < 8; ++i) {
      const float p = __builtin_amdgcn_exp2f(s[i] - mn);
      l += p;
#pragma unroll
      for (int j = 0; j < 4; ++j) { acc[2 * j] = fmaf(p, bflo(Vc[i][j]), acc[2 * j]); acc[2 * j + 1] = fmaf(p, bfhi(Vc[i][j]), acc[2 * j + 1]); }
    }
  }
  float M = m;
  M = fmaxf(M, __shfl_xor(M, 16)); M = fmaxf(M, __shfl_xor(M, 32));
  const float sc = __builtin_amdgcn_exp2f(m - M);
  l *= sc; l += __shfl_xor(l, 16); l += __shfl_xor(l, 32);
#pragma unroll
  for (int j = 0; j < 8; ++j) { float a = acc[j] * sc; a += __shfl_xor(a, 16); a += __shfl_xor(a, 32); acc[j] = a; }
  if (r == 0) {
#pragma unroll
    for (int j = 0; j < 8; ++j) mrg[wave][4 + 8 * c + j] = acc[j];
    if (c == 0) { mrg[wave][0] = M; mrg[wave][1] = l; }
  }
  __syncthreads();
  if (tid < 128) {
    float MM = fmaxf(fmaxf(mrg[0][0], mrg[1][0]), fmaxf(mrg[2][0], mrg[3][0]));
    float L = 0.f, a = 0.f;
#pragma unroll
    for (int w = 0; w < 4; ++w) { const float e = __builtin_amdgcn_exp2f(mrg[w][0] - MM); L += e * mrg[w][1]; a += e * mrg[w][4 + tid]; }
    o[b * HD + h * DH + tid] = f2bf(a / L);
  }
}

extern "C" void kernel_launch(void* const* d_in, const int* in_sizes, int n_in, void* d_out, int out_size, void* d_ws, size_t ws_size, hipStream_t stream) {
  const unsigned short* x = (const unsigned short*)d_in[0];
  const unsigned short* kc = (const unsigned short*)d_in[1];
  const unsigned short* vc = (const unsigned short*)d_in[2];
  const unsigned short* wq = (const unsigned short*)d_in[3];
  const unsigned short* wo = (const unsigned short*)d_in[4];
  unsigned short* q = (unsigned short*)d_ws;
  unsigned short* o = q + B * HD;
  gemm16<D, HD, true><<<HD / 16, 256, 0, stream>>>(x, wq, q);
  attn<<<B * H, 256, 0, stream>>>(q, kc, vc, o);
  gemm16<HD, D, true><<<D / 16, 256, 0, stream>>>(o, wo, (unsigned short*)d_out);
}
```

```cpp
#include <hip/hip_runtime.h>
#include <stdint.h>

typedef unsigned u32x4 __attribute__((ext_vector_type(4)));
typedef unsigned u32x2 __attribute__((ext_vector_type(2)));
typedef float f32x4 __attribute__((ext_vector_type(4)));
typedef float f32x2 __attribute__((ext_vector_type(2)));
typedef __bf16 bf16x2 __attribute__((ext_vector_type(2)));
typedef __bf16 bf16x8 __attribute__((ext_vector_type(8)));
typedef short s16x4 __attribute__((ext_vector_type(4)));
typedef short s16x8 __attribute__((ext_vector_type(8)));
#define LAS __attribute__((address_space(3)))

#define WIN 5
#define AHEAD 48
#define CATCH 4
#define QWIN 2
#define WO_AT 2
#define TRIG (31 - WIN)
#define KAUX 2
#define VAUX 2
#define QSLEEP 4
#define G1SLEEP 2
#define G2SLEEP 8
#define FBACK 1
#define FSLEEP 2

constexpr int NB = 16, DM = 1024, NH = 32, SK = 2048, DH = 128, HD = NH * DH;
constexpr unsigned KV_BYTES = 1u << 28;
constexpr unsigned WS_Q = 4096, Q_STRIDE = 2048;
constexpr unsigned WS_PART = 1u << 20, PART_STRIDE = 4096 + 256;
constexpr unsigned WS_P = 4u << 20, P_STRIDE = 8192;
constexpr unsigned WS_SPAN = 8u << 20;
constexpr unsigned OOB = 0x80000000u;
constexpr unsigned WS_PROF = 16u << 20;
constexpr int L_WQ = 0;
constexpr int L_QP = L_WQ + 4 * 8192;
constexpr int L_Q = L_QP + 4 * 2048;
constexpr int L_ACC = L_Q + 8 * 256;
constexpr int L_HDR = L_ACC + 16 * 4 * 512;
constexpr int O_ROW = 272;
constexpr int L_O = L_HDR + 16 * 4 * 16;
constexpr int L_CNT = L_O + 16 * O_ROW;
constexpr int C_QP = L_CNT, C_Q = L_CNT + 4, C_TRIG = L_CNT + 8, C_O = L_CNT + 12, C_EP = L_CNT + 16;
constexpr int L_END = L_CNT + 64;
constexpr int LDS_BYTES = 96 * 1024;
static_assert(L_END + 1024 <= LDS_BYTES, "lds");
constexpr unsigned SPIN_CAP = 1u << 15;

__device__ __forceinline__ float asf(unsigned u) { return __builtin_bit_cast(float, u); }
__device__ __forceinline__ unsigned asu(float f) { return __builtin_bit_cast(unsigned, f); }
__device__ __forceinline__ float dot2(unsigned a, unsigned b, float acc) {
  return __builtin_amdgcn_fdot2_f32_bf16(__builtin_bit_cast(bf16x2, a), __builtin_bit_cast(bf16x2, b), acc, false);
}
template <int CTRL> __device__ __forceinline__ float dpp(float x) {
  return asf((unsigned)__builtin_amdgcn_mov_dpp((int)asu(x), CTRL, 0xf, 0xf, true));
}
constexpr int QX1 = 0xB1, QX2 = 0x4E, HMIR = 0x141, ROR4 = 0x124, ROR8 = 0x128, BCAST = 0x150;
__device__ __forceinline__ unsigned pack_bf16(float lo, float hi) {
  bf16x2 r; r[0] = (__bf16)lo; r[1] = (__bf16)hi; return __builtin_bit_cast(unsigned, r);
}
__device__ __forceinline__ float fold16(float a, float b) {
  auto r = __builtin_amdgcn_permlane16_swap(asu(a), asu(b), false, false); return asf(r[0]) + asf(r[1]);
}
__device__ __forceinline__ float fold32(float a, float b) {
  auto r = __builtin_amdgcn_permlane32_swap(asu(a), asu(b), false, false); return asf(r[0]) + asf(r[1]);
}
__device__ __forceinline__ float max16(float a) {
  auto r = __builtin_amdgcn_permlane16_swap(asu(a), asu(a), false, false); return fmaxf(asf(r[0]), asf(r[1]));
}
__device__ __forceinline__ float max32(float a) {
  auto r = __builtin_amdgcn_permlane32_swap(asu(a), asu(a), false, false); return fmaxf(asf(r[0]), asf(r[1]));
}
__device__ __forceinline__ __amdgpu_buffer_rsrc_t mkrsrc(const void* p, unsigned bytes) {
  return __builtin_amdgcn_make_buffer_rsrc((void*)p, 0, (int)bytes, 0x00020000);
}
template <typename T> __device__ __forceinline__ LAS T* lds_at(LAS char* s, int off) { return (LAS T*)(s + off); }
#define CBAR() asm volatile("" ::: "memory")
#define SCHED() __builtin_amdgcn_sched_barrier(0)

#define STAMP(slot) do {} while (0)

__device__ __forceinline__ void lds_wait_ge(LAS char* smem, int off, unsigned want) {
#pragma unroll 1
  for (unsigned spins = 0; spins < SPIN_CAP; ++spins) {
    unsigned v = *lds_at<volatile unsigned>(smem, off);
    if ((unsigned)__builtin_amdgcn_readfirstlane((int)v) >= want) break;
    __builtin_amdgcn_s_sleep(1);
  }
  CBAR();
}

template <int SLEEP>
__device__ __forceinline__ void lds_wait_flags(LAS char* smem, int u, int lane) {
#pragma unroll 1
  for (unsigned spins = 0; spins < SPIN_CAP; ++spins) {
    const unsigned f = *lds_at<volatile unsigned>(smem, L_HDR + (u * 4 + (lane & 3)) * 16 + 8);
    if (__builtin_amdgcn_ballot_w64(f != 1u) == 0) break;
    __builtin_amdgcn_s_sleep(SLEEP);
  }
  CBAR();
}

__device__ __forceinline__ bf16x8 colfrag(const u32x4* r, int t) {
  u32x4 f;
#pragma unroll
  for (int u = 0; u < 4; ++u)
    f[u] = __builtin_amdgcn_perm(r[2 * u + 1][t >> 1], r[2 * u][t >> 1], (t & 1) ? 0x07060302u : 0x05040100u);
  return __builtin_bit_cast(bf16x8, f);
}

struct Tag { unsigned t[4]; };
__device__ __forceinline__ u32x4 tag_pack(f32x4 v, const Tag& g) {
  u32x4 r;
#pragma unroll
  for (int i = 0; i < 4; ++i) r[i] = (asu(v[i]) & 0xfffffc00u) | g.t[i];
  return r;
}
__device__ __forceinline__ unsigned tag_bad(u32x4 v, const Tag& g) {
  return ((v[0] ^ g.t[0]) | (v[1] ^ g.t[1]) | (v[2] ^ g.t[2]) | (v[3] ^ g.t[3]));
}
__device__ __forceinline__ f32x4 untag(u32x4 v) {
  f32x4 r;
#pragma unroll
  for (int i = 0; i < 4; ++i) r[i] = asf(v[i] & 0xfffffc00u);
  return r;
}

__device__ __forceinline__ unsigned make_tag(unsigned ep, Tag& tg) {
  const unsigned e1 = (unsigned)__builtin_amdgcn_readfirstlane((int)ep) + 1u;
  tg.t[0] = e1 & 1023u; tg.t[1] = (e1 + (e1 >> 10)) & 1023u; tg.t[2] = (e1 + (e1 >> 20)) & 1023u; tg.t[3] = (e1 + (e1 >> 30) + 341u) & 1023u;
  asm volatile("" :: "s"(e1) : "memory");
  return e1;
}
__device__ __forceinline__ float bcast16(float x, int l) {
  switch (l) {
    case 0: return dpp<BCAST + 0>(x); case 1: return dpp<BCAST + 1>(x); case 2: return dpp<BCAST + 2>(x); case 3: return dpp<BCAST + 3>(x);
    case 4: return dpp<BCAST + 4>(x); case 5: return dpp<BCAST + 5>(x); case 6: return dpp<BCAST + 6>(x); case 7: return dpp<BCAST + 7>(x);
    case 8: return dpp<BCAST + 8>(x); case 9: return dpp<BCAST + 9>(x); case 10: return dpp<BCAST + 10>(x); case 11: return dpp<BCAST + 11>(x);
    case 12: return dpp<BCAST + 12>(x); case 13: return dpp<BCAST + 13>(x); case 14: return dpp<BCAST + 14>(x); default: return dpp<BCAST + 15>(x);
  }
}

struct Stream { __amdgpu_buffer_rsrc_t rk, rv; unsigned voff; };
__device__ __forceinline__ constexpr unsigned unit_off(int u) { return ((unsigned)(u & 1) << 17) | ((unsigned)(u >> 1) << 25); }
__device__ __forceinline__ u32x4 kv_load(const Stream& s, int n, unsigned sb) {
  const int i = n & 15;
  const unsigned so = sb + (unsigned)(((i >> 2) & 1) << 15) + (unsigned)(((i >> 3) & 1) << 14);
  const unsigned vo = s.voff + (unsigned)((i & 3) << 10);
  return (n & 16) ? __builtin_amdgcn_raw_buffer_load_b128(s.rv, vo, so, VAUX) : __builtin_amdgcn_raw_buffer_load_b128(s.rk, vo, so, KAUX);
}

constexpr int issue_upto(int n) {
  return (n + WIN + 1 > AHEAD + WIN + n / CATCH) ? n + WIN + 1 : AHEAD + WIN + n / CATCH;
}
static_assert(issue_upto(63) == 64 + WIN, "catch-up must end within two batches");

__device__ __forceinline__ void lds_inc(LAS char* smem, int off, int lane) {
  CBAR();
  if (lane == 0) __hip_atomic_fetch_add(lds_at<unsigned>(smem, off), 1u, __ATOMIC_RELAXED, __HIP_MEMORY_SCOPE_WORKGROUP);
  CBAR();
}

template <int MODE, int N>
__device__ __forceinline__ void att_batch(const Stream& s, u32x4 (&buf)[N], unsigned sb, unsigned sbn, int b, LAS char* smem, int wave, int lane) {
  constexpr float C2 = 0.088388347648318440f * 1.4426950408889634f;
  const int c = lane & 15, r = lane >> 4;
  const u32x4 qv = *lds_at<u32x4>(smem, L_Q + (b >> 1) * 256 + c * 16);
  const bool b3 = (lane & 8) != 0, b2 = (lane & 4) != 0;
  float a[2], u[2], v[4], p[4];
  f32x2 acc[4];
  float mneg = 0.f;
#pragma unroll
  for (int n = 0; n < 32; ++n) {
    constexpr int OFF = MODE == 1 ? 32 : 0;
    if (MODE >= 2) {
      asm volatile("" :: "v"(buf[n]));
      if (MODE == 2 || n + WIN < 32) buf[(n + WIN) & 31] = kv_load(s, (n + WIN) & 31, n + WIN < 32 ? sb : sbn);
      if (MODE == 3 && n == TRIG) lds_inc(smem, C_TRIG, lane);
    } else {
      const int g = n + OFF;
#pragma unroll
      for (int i = (g == 0 ? AHEAD + WIN : issue_upto(g - 1)); i < issue_upto(g); ++i) {
        asm volatile("" :: "v"(buf[i - WIN]));
        buf[i] = kv_load(s, i & 31, sb + unit_off(i >> 5));
      }
    }
    SCHED();
    const u32x4 d = buf[n + OFF];
    if (n < 16) {
      float t = dot2(d[0], qv[0], 0.f);
      t = dot2(d[1], qv[1], t); t = dot2(d[2], qv[2], t); t = dot2(d[3], qv[3], t);
      a[n & 1] = t;
      if ((n & 1) == 1) {
        const float keep = b3 ? a[1] : a[0], send = b3 ? a[0] : a[1];
        u[(n >> 1) & 1] = keep + dpp<ROR8>(send);
      }
      if ((n & 3) == 3) {
        const float keep = b2 ? u[1] : u[0], send = b2 ? u[0] : u[1];
        float x = keep + dpp<HMIR>(send);
        x += dpp<QX1>(x); x += dpp<QX2>(x);
        v[n >> 2] = x;
      }
      if (n == 15) {
        float m = fmaxf(fmaxf(v[0], v[1]), fmaxf(v[2], v[3]));
        m = fmaxf(m, dpp<ROR4>(m)); m = fmaxf(m, dpp<ROR8>(m));
        m = max16(m); m = max32(m);
        mneg = -m * C2;
#pragma unroll
        for (int k = 0; k < 4; ++k) p[k] = __builtin_amdgcn_exp2f(fmaf(v[k], C2, mneg));
      }
    } else {
      const int i = n - 16;
      float pb;
      switch (i & 3) {
        case 0: pb = dpp<BCAST + 0>(p[i >> 2]); break;
        case 1: pb = dpp<BCAST + 8>(p[i >> 2]); break;
        case 2: pb = dpp<BCAST + 4>(p[i >> 2]); break;
        default: pb = dpp<BCAST + 12>(p[i >> 2]); break;
      }
      const f32x2 pp = {pb, pb};
#pragma unroll
      for (int j = 0; j < 4; ++j) {
        const f32x2 x = {asf(d[j] << 16), asf(d[j] & 0xffff0000u)};
        acc[j] = (i == 0) ? pp * x : __builtin_elementwise_fma(pp, x, acc[j]);
      }
    }
    SCHED();
  }
  float l = (p[0] + p[1]) + (p[2] + p[3]);
  l += dpp<ROR4>(l); l += dpp<ROR8>(l);
  l = fold16(l, l); l = fold32(l, l);
  f32x2 o;
  o[0] = fold32(fold16(acc[0][0], acc[1][0]), fold16(acc[2][0], acc[3][0]));
  o[1] = fold32(fold16(acc[0][1], acc[1][1]), fold16(acc[2][1], acc[3][1]));
  *lds_at<volatile f32x2>(smem, L_ACC + (b * 4 + wave) * 512 + c * 32 + r * 8) = o;
  CBAR();
  if (lane == 0) {
    u32x4 h = {asu(-mneg), asu(l), 1u, 0u};
    *lds_at<volatile u32x4>(smem, L_HDR + (b * 4 + wave) * 16) = h;
  }
  CBAR();
}

template <int SLEEP>
__device__ __forceinline__ void merge8(LAS char* smem, int pp, int lane, f32x4& o, float& lse) {
  u32x4 hd; f32x4 pa[8];
  if (SLEEP) lds_wait_flags<SLEEP>(smem, 2 * pp + 1, lane);
#pragma unroll 1
  for (unsigned spins = 0; spins < SPIN_CAP; ++spins) {
    hd = *lds_at<volatile u32x4>(smem, L_HDR + (pp * 8 + (lane & 7)) * 16);
#pragma unroll
    for (int w = 0; w < 8; ++w) pa[w] = *lds_at<volatile f32x4>(smem, L_ACC + (pp * 8 + w) * 512 + (lane & 31) * 16);
    if (__builtin_amdgcn_ballot_w64(hd[2] != 1u) == 0) break;
  }
  const float m = asf(hd[0]);
  float M = fmaxf(m, dpp<QX1>(m)); M = fmaxf(M, dpp<QX2>(M)); M = fmaxf(M, dpp<HMIR>(M));
  const float e = __builtin_amdgcn_exp2f(m - M);
  float L = e * asf(hd[1]); L += dpp<QX1>(L); L += dpp<QX2>(L); L += dpp<HMIR>(L);
  const float en = e * __builtin_amdgcn_rcpf(L);
  o = pa[0] * bcast16(en, 0);
#pragma unroll
  for (int w = 1; w < 8; ++w) o += pa[w] * bcast16(en, w);
  lse = M + __builtin_amdgcn_logf(L);
}
__device__ __forceinline__ f32x4 lse_chunk(float lse) {
  const float hi = asf(asu(lse) & 0xfffffc00u);
  return f32x4{hi, lse - hi, 0.f, 0.f};
}

template <int N, int SLEEP, int BACK>
__device__ __forceinline__ void gather(u32x4 (&v)[N], __amdgpu_buffer_rsrc_t rws, unsigned (&off)[N], const unsigned (&so)[N], const Tag& tg) {
#pragma unroll
  for (int n = 0; n < N; ++n) v[n] = u32x4{0u, 0u, 0u, 0u};
#pragma unroll 1
  for (unsigned spins = 0; spins < SPIN_CAP; ++spins) {
    CBAR();
    u32x4 t[N];
#pragma unroll
    for (int n = 0; n < N; ++n) t[n] = __builtin_amdgcn_raw_buffer_load_b128(rws, off[n], so[n], 16);
    int left = 0;
#pragma unroll
    for (int n = 0; n < N; ++n) {
      const bool want = off[n] != OOB;
      if (want) v[n] = t[n];
      const bool miss = want && (tag_bad(t[n], tg) & 1023u) != 0;
      off[n] = miss ? off[n] : OOB;
      left += __builtin_popcountll(__builtin_amdgcn_ballot_w64(miss));
    }
    if (left == 0) break;
    if (SLEEP) __builtin_amdgcn_s_sleep(SLEEP);
    if (BACK) for (int i = left >> 4; i > 0; --i) __builtin_amdgcn_s_sleep(BACK);
  }
}

__global__ void __launch_bounds__(512, 2)
mha_fused(const char* __restrict__ x, const char* __restrict__ kc, const char* __restrict__ vc,
          const char* __restrict__ wq, const char* __restrict__ wo, char* __restrict__ out, char* __restrict__ ws) {
  extern __shared__ char smem_raw[];
  LAS char* smem = (LAS char*)smem_raw;
  const int lane = threadIdx.x & 63;
  const int wave = __builtin_amdgcn_readfirstlane(threadIdx.x >> 6);
  const int blk = blockIdx.x;
  const int h = (blk & 7) * 4 + (blk >> 6), j = (blk >> 3) & 7;
  const int par = (j >> 1) & 1;
  STAMP(0);

  if (wave < 4) {
    Stream s;
    s.rk = mkrsrc(kc, KV_BYTES); s.rv = mkrsrc(vc, KV_BYTES); s.voff = lane * 16;
    const unsigned base = ((unsigned)h << 19) | ((unsigned)(j & 5) << 16) | ((unsigned)par << 24) | ((unsigned)wave << 12);
    u32x4 ring[32];
    {
      u32x4 buf[64 + WIN];
#pragma unroll
      for (int n = 0; n < WIN; ++n) buf[n] = kv_load(s, n, base);
      SCHED();
      __builtin_amdgcn_s_barrier();
      SCHED();
#pragma unroll
      for (int n = 0; n < AHEAD; ++n) {
        asm volatile("" :: "v"(buf[n]));
        buf[n + WIN] = kv_load(s, (n + WIN) & 31, base + unit_off((n + WIN) >> 5));
        SCHED();
      }
      STAMP(1);
      lds_wait_ge(smem, C_Q, 1);
      STAMP(2);
      att_batch<0>(s, buf, base, 0, 0, smem, wave, lane);
      STAMP(3);
      att_batch<1>(s, buf, base, 0, 1, smem, wave, lane);
      STAMP(4);
#pragma unroll
      for (int n = 0; n < WIN; ++n) ring[n] = buf[64 + n];
    }
    lds_wait_ge(smem, C_Q, 2);
    for (int u = 2; u < 15; ++u) {
      att_batch<2>(s, ring, base + unit_off(u), base + unit_off(u + 1), u, smem, wave, lane);
      STAMP(3 + u);
    }
    att_batch<3>(s, ring, base + unit_off(15), 0, 15, smem, wave, lane);
    STAMP(18);
  } else {
    const int sw = wave - 4;
    const int c = lane & 15, g = lane >> 4, half = lane >> 5;
    const __amdgpu_buffer_rsrc_t rws = mkrsrc(ws, WS_SPAN);
    Tag tg;
    unsigned e1;
    {
      const __amdgpu_buffer_rsrc_t rq = mkrsrc(wq, DM * HD * 2), rx = mkrsrc(x, NB * DM * 2);
      const int row0 = 512 * (j >> 2) + 128 * sw;
      u32x4 wb[8], xf[4];
#pragma unroll
      for (int i = 0; i < 8; ++i)
        wb[i] = __builtin_amdgcn_raw_buffer_load_b128(rq, (lane >> 2) * (HD * 2) + (lane & 3) * 16, (row0 + 16 * i) * (HD * 2) + h * 256 + (j & 3) * 64, 2);
#pragma unroll
      for (int k = 0; k < 4; ++k) xf[k] = __builtin_amdgcn_raw_buffer_load_b128(rx, c * (DM * 2) + g * 16 + k * 64, row0 * 2, 0);
      const unsigned ep = __builtin_amdgcn_raw_buffer_load_b32(rws, 0, 0, 16);
      SCHED();
      if (sw == 0) {
        if (lane < 8) *lds_at<volatile unsigned>(smem, L_CNT + lane * 4) = 0u;
        *lds_at<volatile u32x4>(smem, L_HDR + lane * 16) = u32x4{0u, 0u, 0u, 0u};
        asm volatile("s_waitcnt lgkmcnt(0)" ::: "memory");
      }
      __builtin_amdgcn_s_barrier();
      SCHED();
#pragma unroll
      for (int i = 0; i < 8; ++i) *lds_at<volatile u32x4>(smem, L_WQ + sw * 8192 + i * 1024 + lane * 16) = wb[i];
      CBAR();
      f32x4 acc[2] = {f32x4{0.f, 0.f, 0.f, 0.f}, f32x4{0.f, 0.f, 0.f, 0.f}};
#pragma unroll
      for (int k = 0; k < 4; ++k)
#pragma unroll
        for (int t = 0; t < 2; ++t) {
          const int ad = L_WQ + sw * 8192 + (32 * k + 8 * g + (c >> 2)) * 64 + t * 32 + (c & 3) * 8;
          const s16x4 lo = __builtin_amdgcn_ds_read_tr16_b64_v4i16(lds_at<s16x4>(smem, ad));
          const s16x4 hi = __builtin_amdgcn_ds_read_tr16_b64_v4i16(lds_at<s16x4>(smem, ad + 256));
          const s16x8 fr = __builtin_shufflevector(lo, hi, 0, 1, 2, 3, 4, 5, 6, 7);
          acc[t] = __builtin_amdgcn_mfma_f32_16x16x32_bf16(__builtin_bit_cast(bf16x8, fr), __builtin_bit_cast(bf16x8, xf[k]), acc[t], 0, 0, 0);
        }
      *lds_at<volatile f32x4>(smem, L_QP + sw * 2048 + lane * 16) = acc[0];
      *lds_at<volatile f32x4>(smem, L_QP + sw * 2048 + 1024 + lane * 16) = acc[1];
      e1 = make_tag(ep, tg);
      lds_inc(smem, C_QP, lane);
      STAMP(1);
      if (sw < 2) {
        lds_wait_ge(smem, C_QP, 4);
        f32x4 sum = {0.f, 0.f, 0.f, 0.f};
#pragma unroll
        for (int w = 0; w < 4; ++w) sum += *lds_at<volatile f32x4>(smem, L_QP + w * 2048 + ((lane & 7) * 16 + 8 * sw + (lane >> 3)) * 16);
        __builtin_amdgcn_raw_buffer_store_b128(tag_pack(sum, tg), rws, lane * 16, WS_Q + (h * 8 + j) * Q_STRIDE + sw * 1024, 16);
      }
    }
    const unsigned grp = WS_PART + h * 8 * PART_STRIDE;
    if (sw == 0) {
      {
        const unsigned qo = (lane >> 3) * Q_STRIDE + (lane & 7) * 16, qs = WS_Q + h * 8 * Q_STRIDE + par * 128;
        {
          u32x4 v[1]; unsigned off[1] = {qo}; const unsigned so[1] = {qs};
          gather<1, QSLEEP, 0>(v, rws, off, so, tg);
          const f32x4 f = untag(v[0]);
          *lds_at<volatile unsigned>(smem, L_Q + (lane & 31) * 8 + half * 4) = pack_bf16(fold32(f[0], f[2]), fold32(f[1], f[3]));
          lds_inc(smem, C_Q, lane);
        }
        STAMP(2);
        u32x4 v[7]; unsigned off[7], so[7];
#pragma unroll
        for (int n = 0; n < 7; ++n) { off[n] = qo; so[n] = qs + (n + 1) * 256; }
        gather<7, QSLEEP, 0>(v, rws, off, so, tg);
#pragma unroll
        for (int n = 0; n < 7; ++n) {
          const f32x4 f = untag(v[n]);
          *lds_at<volatile unsigned>(smem, L_Q + (n + 1) * 256 + (lane & 31) * 8 + half * 4) = pack_bf16(fold32(f[0], f[2]), fold32(f[1], f[3]));
        }
        lds_inc(smem, C_Q, lane);
        STAMP(3);
      }
      u32x4 msg[4]; f32x4 lch = {0.f, 0.f, 0.f, 0.f};
#pragma unroll
      for (int i = 0; i < 4; ++i) {
        f32x4 o; float lse;
        merge8<16>(smem, i < 3 ? 2 * i + half : 6, lane, o, lse);
        msg[i] = tag_pack(o, tg);
        if ((lane & 31) == i) lch = lse_chunk(lse);
        asm volatile("" : "+v"(msg[i]), "+v"(lch));
      }
      const unsigned mine = grp + (par * 4 + (j & 1) + 2 * (j >> 2)) * PART_STRIDE;
      lds_wait_flags<16>(smem, 14, lane);
      lds_wait_ge(smem, C_TRIG, 4);
#pragma unroll
      for (int i = 0; i < 4; ++i)
        __builtin_amdgcn_raw_buffer_store_b128(msg[i], rws, (i < 3 || lane < 32) ? lane * 16u : OOB, mine + i * 1024, 16);
      __builtin_amdgcn_raw_buffer_store_b128(tag_pack(lch, tg), rws, (lane & 31) < 4 ? ((lane & 31) * 2 + half) * 16u : OOB, mine + 4096, 16);
      STAMP(17);
      {
        f32x4 o; float lse;
        merge8<0>(smem, 7, lane, o, lse);
        if (lane >= 32) o = lse_chunk(lse);
        __builtin_amdgcn_raw_buffer_store_b128(tag_pack(o, tg), rws, lane < 32 ? lane * 16u : lane < 40 ? lane * 16u + 128u : OOB, mine + 7 * 512, 16);
      }
      STAMP(18);
      lds_wait_ge(smem, C_O, 3);
      u32x4 v[8]; unsigned off[8], so[8];
#pragma unroll
      for (int n = 0; n < 8; ++n) { off[n] = g * P_STRIDE + c * 16; so[n] = WS_P + (j * NH + 4 * n) * P_STRIDE + h * 256; }
      gather<8, FSLEEP, FBACK>(v, rws, off, so, tg);
      f32x4 sum = untag(v[0]);
#pragma unroll
      for (int n = 1; n < 8; ++n) sum += untag(v[n]);
      const float y = fold32(fold16(sum[0], sum[1]), fold16(sum[2], sum[3]));
      auto sw16 = __builtin_amdgcn_permlane16_swap(asu(y), asu(y), false, false);
      const unsigned pk = pack_bf16(asf(sw16[0]), asf(sw16[1]));
      if ((g & 1) == 0) *(unsigned*)(out + c * (DM * 2) + (j * 128 + 32 * (h & 3) + 4 * (h >> 2) + g) * 2) = pk;
      if (blk == 0 && lane == 0) __builtin_amdgcn_raw_buffer_store_b32(e1, rws, 0, 0, 16);
      STAMP(20);
    } else if (sw == 1) {
      const __amdgpu_buffer_rsrc_t ro = mkrsrc(wo, HD * DM * 2);
      bf16x8 af[4][8];
      lds_wait_flags<16>(smem, WO_AT, lane);
#pragma unroll
      for (int k = 0; k < 4; ++k) {
        u32x4 wb[8];
#pragma unroll
        for (int i = 0; i < 8; ++i)
          wb[i] = __builtin_amdgcn_raw_buffer_load_b128(ro, g * (8 * DM * 2) + c * 16, (h * DH + k * 32 + i) * (DM * 2) + j * 256, 2);
#pragma unroll
        for (int t = 0; t < 8; ++t) { af[k][t] = colfrag(wb, t); asm volatile("" : "+v"(af[k][t])); }
        SCHED();
      }
      lds_wait_flags<16>(smem, 14, lane);
      lds_wait_flags<1>(smem, 15, lane);
      u32x4 v[5]; unsigned off[5], so[5];
#pragma unroll
      for (int i = 0; i < 4; ++i) { off[i] = half * 4 * PART_STRIDE + (lane & 31) * 16; so[i] = grp + i * PART_STRIDE + 7 * 512; }
      off[4] = (half * 4 + (lane & 3)) * PART_STRIDE; so[4] = grp + 4096 + 128;
      gather<5, G1SLEEP, 0>(v, rws, off, so, tg);
      const f32x4 lf = untag(v[4]);
      float M = fmaxf(lf[0], dpp<QX1>(lf[0])); M = fmaxf(M, dpp<QX2>(M));
      const float e = __builtin_amdgcn_exp2f((lf[0] - M) + lf[1]);
      float S = e + dpp<QX1>(e); S += dpp<QX2>(S);
      const float en = e * __builtin_amdgcn_rcpf(S);
      const f32x4 o = untag(v[0]) * dpp<0x00>(en) + untag(v[1]) * dpp<0x55>(en) + untag(v[2]) * dpp<0xAA>(en) + untag(v[3]) * dpp<0xFF>(en);
      *lds_at<volatile u32x2>(smem, L_O + (14 + half) * O_ROW + (lane & 31) * 8) = u32x2{pack_bf16(o[0], o[1]), pack_bf16(o[2], o[3])};
      STAMP(18);
      lds_wait_ge(smem, C_O, 2);
      STAMP(19);
      u32x4 of[4];
#pragma unroll
      for (int k = 0; k < 4; ++k) of[k] = *lds_at<volatile u32x4>(smem, L_O + c * O_ROW + k * 64 + g * 16);
      SCHED();
      f32x4 acc[8];
#pragma unroll
      for (int k = 0; k < 4; ++k)
#pragma unroll
        for (int t = 0; t < 8; ++t)
          acc[t] = __builtin_amdgcn_mfma_f32_16x16x32_bf16(af[k][t], __builtin_bit_cast(bf16x8, of[k]), k == 0 ? f32x4{0.f, 0.f, 0.f, 0.f} : acc[t], 0, 0, 0);
#pragma unroll
      for (int n = 0; n < 8; ++n) {
        const int i = n >> 1, th = n & 1;
        const f32x4 pv = {acc[4 * th][i], acc[4 * th + 1][i], acc[4 * th + 2][i], acc[4 * th + 3][i]};
        __builtin_amdgcn_raw_buffer_store_b128(tag_pack(pv, tg), rws, lane * 16, WS_P + (j * NH + h) * P_STRIDE + n * 1024, 16);
      }
      lds_inc(smem, C_O, lane);
      STAMP(20);
    } else {
      const int p0 = (sw - 2) * 4;
      lds_wait_flags<16>(smem, 14, lane);
      lds_wait_ge(smem, C_TRIG, 4);
      u32x4 lv[1];
      {
        unsigned off[1] = {(half * 4 + (lane & 3)) * PART_STRIDE + ((lane >> 2) & 3) * 16}; const unsigned so[1] = {grp + 4096 + p0 * 16};
        gather<1, G2SLEEP, 0>(lv, rws, off, so, tg);
      }
      STAMP(17);
      u32x4 v[16]; unsigned off[16], so[16];
#pragma unroll
      for (int n = 0; n < 16; ++n) {
        off[n] = (n >= 12 && sw == 3) ? OOB : half * 4 * PART_STRIDE + (lane & 31) * 16;
        so[n] = grp + (n & 3) * PART_STRIDE + (p0 + (n >> 2)) * 512;
      }
      gather<16, 2, 0>(v, rws, off, so, tg);
      const f32x4 lf = untag(lv[0]);
      float M = fmaxf(lf[0], dpp<QX1>(lf[0])); M = fmaxf(M, dpp<QX2>(M));
      const float e = __builtin_amdgcn_exp2f((lf[0] - M) + lf[1]);
      float S = e + dpp<QX1>(e); S += dpp<QX2>(S);
      const float en = e * __builtin_amdgcn_rcpf(S);
#pragma unroll
      for (int p = 0; p < 4; ++p) {
        f32x4 o = untag(v[4 * p]) * bcast16(en, 4 * p);
#pragma unroll
        for (int m = 1; m < 4; ++m) o += untag(v[4 * p + m]) * bcast16(en, 4 * p + m);
        if (!(p == 3 && sw == 3))
          *lds_at<volatile u32x2>(smem, L_O + (2 * (p0 + p) + half) * O_ROW + (lane & 31) * 8) = u32x2{pack_bf16(o[0], o[1]), pack_bf16(o[2], o[3])};
      }
      lds_inc(smem, C_O, lane);
      STAMP(18);
    }
  }
}

extern "C" void kernel_launch(void* const* d_in, const int* in_sizes, int n_in, void* d_out, int out_size, void* d_ws, size_t ws_size, hipStream_t stream) {
  (void)hipFuncSetAttribute((const void*)mha_fused, hipFuncAttributeMaxDynamicSharedMemorySize, LDS_BYTES);
  mha_fused<<<256, 512, LDS_BYTES, stream>>>((const char*)d_in[0], (const char*)d_in[1], (const char*)d_in[2],
                                             (const char*)d_in[3], (const char*)d_in[4], (char*)d_out, (char*)d_ws);
}
```

```cpp
#define PROBE 1
#define PBLK 100
#define PW1 0
#define PS1 0
#define PW0 0
#define PS0 0
#define PM 0
#include <hip/hip_runtime.h>
#include <stdint.h>

typedef unsigned u32x4 __attribute__((ext_vector_type(4)));
typedef unsigned u32x2 __attribute__((ext_vector_type(2)));
typedef float f32x4 __attribute__((ext_vector_type(4)));
typedef float f32x2 __attribute__((ext_vector_type(2)));
typedef __bf16 bf16x2 __attribute__((ext_vector_type(2)));
typedef __bf16 bf16x8 __attribute__((ext_vector_type(8)));
typedef short s16x4 __attribute__((ext_vector_type(4)));
typedef short s16x8 __attribute__((ext_vector_type(8)));
#define LAS __attribute__((address_space(3)))

#define WIN 5
#define AHEAD 48
#define CATCH 4
#define QWIN 2
#define WO_AT 2
#define TRIG (31 - WIN)
#define KAUX 2
#define VAUX 2
#define QSLEEP 4
#define G1SLEEP 2
#define G2SLEEP 8
#define FBACK 1
#define FSLEEP 2

constexpr int NB = 16, DM = 1024, NH = 32, SK = 2048, DH = 128, HD = NH * DH;
constexpr unsigned KV_BYTES = 1u << 28;
constexpr unsigned WS_Q = 4096, Q_STRIDE = 2048;
constexpr unsigned WS_PART = 1u << 20, PART_STRIDE = 4096 + 256;
constexpr unsigned WS_P = 4u << 20, P_STRIDE = 8192;
constexpr unsigned WS_SPAN = 8u << 20;
constexpr unsigned OOB = 0x80000000u;
constexpr unsigned WS_PROF = 16u << 20;
constexpr int L_WQ = 0;
constexpr int L_QP = L_WQ + 4 * 8192;
constexpr int L_Q = L_QP + 4 * 2048;
constexpr int L_ACC = L_Q + 8 * 256;
constexpr int L_HDR = L_ACC + 16 * 4 * 512;
constexpr int O_ROW = 272;
constexpr int L_O = L_HDR + 16 * 4 * 16;
constexpr int L_CNT = L_O + 16 * O_ROW;
constexpr int C_QP = L_CNT, C_Q = L_CNT + 4, C_TRIG = L_CNT + 8, C_O = L_CNT + 12, C_EP = L_CNT + 16;
constexpr int L_END = L_CNT + 64;
constexpr int LDS_BYTES = 96 * 1024;
static_assert(L_END + 1024 <= LDS_BYTES, "lds");
constexpr unsigned SPIN_CAP = 1u << 15;

__device__ __forceinline__ float asf(unsigned u) { return __builtin_bit_cast(float, u); }
__device__ __forceinline__ unsigned asu(float f) { return __builtin_bit_cast(unsigned, f); }
__device__ __forceinline__ float dot2(unsigned a, unsigned b, float acc) {
  return __builtin_amdgcn_fdot2_f32_bf16(__builtin_bit_cast(bf16x2, a), __builtin_bit_cast(bf16x2, b), acc, false);
}
template <int CTRL> __device__ __forceinline__ float dpp(float x) {
  return asf((unsigned)__builtin_amdgcn_mov_dpp((int)asu(x), CTRL, 0xf, 0xf, true));
}
constexpr int QX1 = 0xB1, QX2 = 0x4E, HMIR = 0x141, ROR4 = 0x124, ROR8 = 0x128, BCAST = 0x150;
__device__ __forceinline__ unsigned pack_bf16(float lo, float hi) {
  bf16x2 r; r[0] = (__bf16)lo; r[1] = (__bf16)hi; return __builtin_bit_cast(unsigned, r);
}
__device__ __forceinline__ float fold16(float a, float b) {
  auto r = __builtin_amdgcn_permlane16_swap(asu(a), asu(b), false, false); return asf(r[0]) + asf(r[1]);
}
__device__ __forceinline__ float fold32(float a, float b) {
  auto r = __builtin_amdgcn_permlane32_swap(asu(a), asu(b), false, false); return asf(r[0]) + asf(r[1]);
}
__device__ __forceinline__ float max16(float a) {
  auto r = __builtin_amdgcn_permlane16_swap(asu(a), asu(a), false, false); return fmaxf(asf(r[0]), asf(r[1]));
}
__device__ __forceinline__ float max32(float a) {
  auto r = __builtin_amdgcn_permlane32_swap(asu(a), asu(a), false, false); return fmaxf(asf(r[0]), asf(r[1]));
}
__device__ __forceinline__ __amdgpu_buffer_rsrc_t mkrsrc(const void* p, unsigned bytes) {
  return __builtin_amdgcn_make_buffer_rsrc((void*)p, 0, (int)bytes, 0x00020000);
}
template <typename T> __device__ __forceinline__ LAS T* lds_at(LAS char* s, int off) { return (LAS T*)(s + off); }
#define CBAR() asm volatile("" ::: "memory")
#define SCHED() __builtin_amdgcn_sched_barrier(0)

#define STAMP(slot) do { unsigned t_ = (unsigned)__builtin_amdgcn_s_memrealtime(); if (lane == 0) *lds_at<volatile unsigned>(smem, L_END + (wave * 32 + (slot)) * 4) = t_; } while (0)

__device__ __forceinline__ void lds_wait_ge(LAS char* smem, int off, unsigned want) {
#pragma unroll 1
  for (unsigned spins = 0; spins < SPIN_CAP; ++spins) {
    unsigned v = *lds_at<volatile unsigned>(smem, off);
    if ((unsigned)__builtin_amdgcn_readfirstlane((int)v) >= want) break;
    __builtin_amdgcn_s_sleep(1);
  }
  CBAR();
}

template <int SLEEP>
__device__ __forceinline__ void lds_wait_flags(LAS char* smem, int u, int lane) {
#pragma unroll 1
  for (unsigned spins = 0; spins < SPIN_CAP; ++spins) {
    const unsigned f = *lds_at<volatile unsigned>(smem, L_HDR + (u * 4 + (lane & 3)) * 16 + 8);
    if (__builtin_amdgcn_ballot_w64(f != 1u) == 0) break;
    __builtin_amdgcn_s_sleep(SLEEP);
  }
  CBAR();
}

__device__ __forceinline__ bf16x8 colfrag(const u32x4* r, int t) {
  u32x4 f;
#pragma unroll
  for (int u = 0; u < 4; ++u)
    f[u] = __builtin_amdgcn_perm(r[2 * u + 1][t >> 1], r[2 * u][t >> 1], (t & 1) ? 0x07060302u : 0x05040100u);
  return __builtin_bit_cast(bf16x8, f);
}

struct Tag { unsigned t[4]; };
__device__ __forceinline__ u32x4 tag_pack(f32x4 v, const Tag& g) {
  u32x4 r;
#pragma unroll
  for (int i = 0; i < 4; ++i) r[i] = (asu(v[i]) & 0xfffffc00u) | g.t[i];
  return r;
}
__device__ __forceinline__ unsigned tag_bad(u32x4 v, const Tag& g) {
  return ((v[0] ^ g.t[0]) | (v[1] ^ g.t[1]) | (v[2] ^ g.t[2]) | (v[3] ^ g.t[3]));
}
__device__ __forceinline__ f32x4 untag(u32x4 v) {
  f32x4 r;
#pragma unroll
  for (int i = 0; i < 4; ++i) r[i] = asf(v[i] & 0xfffffc00u);
  return r;
}

__device__ __forceinline__ unsigned make_tag(unsigned ep, Tag& tg) {
  const unsigned e1 = (unsigned)__builtin_amdgcn_readfirstlane((int)ep) + 1u;
  tg.t[0] = e1 & 1023u; tg.t[1] = (e1 + (e1 >> 10)) & 1023u; tg.t[2] = (e1 + (e1 >> 20)) & 1023u; tg.t[3] = (e1 + (e1 >> 30) + 341u) & 1023u;
  asm volatile("" :: "s"(e1) : "memory");
  return e1;
}
__device__ __forceinline__ float bcast16(float x, int l) {
  switch (l) {
    case 0: return dpp<BCAST + 0>(x); case 1: return dpp<BCAST + 1>(x); case 2: return dpp<BCAST + 2>(x); case 3: return dpp<BCAST + 3>(x);
    case 4: return dpp<BCAST + 4>(x); case 5: return dpp<BCAST + 5>(x); case 6: return dpp<BCAST + 6>(x); case 7: return dpp<BCAST + 7>(x);
    case 8: return dpp<BCAST + 8>(x); case 9: return dpp<BCAST + 9>(x); case 10: return dpp<BCAST + 10>(x); case 11: return dpp<BCAST + 11>(x);
    case 12: return dpp<BCAST + 12>(x); case 13: return dpp<BCAST + 13>(x); case 14: return dpp<BCAST + 14>(x); default: return dpp<BCAST + 15>(x);
  }
}

struct Stream { __amdgpu_buffer_rsrc_t rk, rv; unsigned voff; };
__device__ __forceinline__ constexpr unsigned unit_off(int u) { return ((unsigned)(u & 1) << 17) | ((unsigned)(u >> 1) << 25); }
__device__ __forceinline__ u32x4 kv_load(const Stream& s, int n, unsigned sb) {
  const int i = n & 15;
  const unsigned so = sb + (unsigned)(((i >> 2) & 1) << 15) + (unsigned)(((i >> 3) & 1) << 14);
  const unsigned vo = s.voff + (unsigned)((i & 3) << 10);
  return (n & 16) ? __builtin_amdgcn_raw_buffer_load_b128(s.rv, vo, so, VAUX) : __builtin_amdgcn_raw_buffer_load_b128(s.rk, vo, so, KAUX);
}

constexpr int issue_upto(int n) {
  return (n + WIN + 1 > AHEAD + WIN + n / CATCH) ? n + WIN + 1 : AHEAD + WIN + n / CATCH;
}
static_assert(issue_upto(63) == 64 + WIN, "catch-up must end within two batches");

__device__ __forceinline__ void lds_inc(LAS char* smem, int off, int lane) {
  CBAR();
  if (lane == 0) __hip_atomic_fetch_add(lds_at<unsigned>(smem, off), 1u, __ATOMIC_RELAXED, __HIP_MEMORY_SCOPE_WORKGROUP);
  CBAR();
}

template <int MODE, int N>
__device__ __forceinline__ void att_batch(const Stream& s, u32x4 (&buf)[N], unsigned sb, unsigned sbn, int b, LAS char* smem, int wave, int lane) {
  constexpr float C2 = 0.088388347648318440f * 1.4426950408889634f;
  const int c = lane & 15, r = lane >> 4;
  const u32x4 qv = *lds_at<u32x4>(smem, L_Q + (b >> 1) * 256 + c * 16);
  const bool b3 = (lane & 8) != 0, b2 = (lane & 4) != 0;
  float a[2], u[2], v[4], p[4];
  f32x2 acc[4];
  float mneg = 0.f;
#pragma unroll
  for (int n = 0; n < 32; ++n) {
    constexpr int OFF = MODE == 1 ? 32 : 0;
    if (MODE >= 2) {
      asm volatile("" :: "v"(buf[n]));
      if (MODE == 2 || n + WIN < 32) buf[(n + WIN) & 31] = kv_load(s, (n + WIN) & 31, n + WIN < 32 ? sb : sbn);
      if (MODE == 3 && n == TRIG) lds_inc(smem, C_TRIG, lane);
    } else {
      const int g = n + OFF;
#pragma unroll
      for (int i = (g == 0 ? AHEAD + WIN : issue_upto(g - 1)); i < issue_upto(g); ++i) {
        asm volatile("" :: "v"(buf[i - WIN]));
        buf[i] = kv_load(s, i & 31, sb + unit_off(i >> 5));
      }
    }
    SCHED();
    const u32x4 d = buf[n + OFF];
    if (n < 16) {
      float t = dot2(d[0], qv[0], 0.f);
      t = dot2(d[1], qv[1], t); t = dot2(d[2], qv[2], t); t = dot2(d[3], qv[3], t);
      a[n & 1] = t;
      if ((n & 1) == 1) {
        const float keep = b3 ? a[1] : a[0], send = b3 ? a[0] : a[1];
        u[(n >> 1) & 1] = keep + dpp<ROR8>(send);
      }
      if ((n & 3) == 3) {
        const float keep = b2 ? u[1] : u[0], send = b2 ? u[0] : u[1];
        float x = keep + dpp<HMIR>(send);
        x += dpp<QX1>(x); x += dpp<QX2>(x);
        v[n >> 2] = x;
      }
      if (n == 15) {
        float m = fmaxf(fmaxf(v[0], v[1]), fmaxf(v[2], v[3]));
        m = fmaxf(m, dpp<ROR4>(m)); m = fmaxf(m, dpp<ROR8>(m));
        m = max16(m); m = max32(m);
        mneg = -m * C2;
#pragma unroll
        for (int k = 0; k < 4; ++k) p[k] = __builtin_amdgcn_exp2f(fmaf(v[k], C2, mneg));
      }
    } else {
      const int i = n - 16;
      float pb;
      switch (i & 3) {
        case 0: pb = dpp<BCAST + 0>(p[i >> 2]); break;
        case 1: pb = dpp<BCAST + 8>(p[i >> 2]); break;
        case 2: pb = dpp<BCAST + 4>(p[i >> 2]); break;
        default: pb = dpp<BCAST + 12>(p[i >> 2]); break;
      }
      const f32x2 pp = {pb, pb};
#pragma unroll
      for (int j = 0; j < 4; ++j) {
        const f32x2 x = {asf(d[j] << 16), asf(d[j] & 0xffff0000u)};
        acc[j] = (i == 0) ? pp * x : __builtin_elementwise_fma(pp, x, acc[j]);
      }
    }
    SCHED();
  }
  float l = (p[0] + p[1]) + (p[2] + p[3]);
  l += dpp<ROR4>(l); l += dpp<ROR8>(l);
  l = fold16(l, l); l = fold32(l, l);
  f32x2 o;
  o[0] = fold32(fold16(acc[0][0], acc[1][0]), fold16(acc[2][0], acc[3][0]));
  o[1] = fold32(fold16(acc[0][1], acc[1][1]), fold16(acc[2][1], acc[3][1]));
  *lds_at<volatile f32x2>(smem, L_ACC + (b * 4 + wave) * 512 + c * 32 + r * 8) = o;
  CBAR();
  if (lane == 0) {
    u32x4 h = {asu(-mneg), asu(l), 1u, 0u};
    *lds_at<volatile u32x4>(smem, L_HDR + (b * 4 + wave) * 16) = h;
  }
  CBAR();
}

template <int SLEEP>
__device__ __forceinline__ void merge8(LAS char* smem, int pp, int lane, f32x4& o, float& lse) {
  u32x4 hd; f32x4 pa[8];
  if (SLEEP) lds_wait_flags<SLEEP>(smem, 2 * pp + 1, lane);
#pragma unroll 1
  for (unsigned spins = 0; spins < SPIN_CAP; ++spins) {
    hd = *lds_at<volatile u32x4>(smem, L_HDR + (pp * 8 + (lane & 7)) * 16);
#pragma unroll
    for (int w = 0; w < 8; ++w) pa[w] = *lds_at<volatile f32x4>(smem, L_ACC + (pp * 8 + w) * 512 + (lane & 31) * 16);
    if (__builtin_amdgcn_ballot_w64(hd[2] != 1u) == 0) break;
  }
  const float m = asf(hd[0]);
  float M = fmaxf(m, dpp<QX1>(m)); M = fmaxf(M, dpp<QX2>(M)); M = fmaxf(M, dpp<HMIR>(M));
  const float e = __builtin_amdgcn_exp2f(m - M);
  float L = e * asf(hd[1]); L += dpp<QX1>(L); L += dpp<QX2>(L); L += dpp<HMIR>(L);
  const float en = e * __builtin_amdgcn_rcpf(L);
  o = pa[0] * bcast16(en, 0);
#pragma unroll
  for (int w = 1; w < 8; ++w) o += pa[w] * bcast16(en, w);
  lse = M + __builtin_amdgcn_logf(L);
}
__device__ __forceinline__ f32x4 lse_chunk(float lse) {
  const float hi = asf(asu(lse) & 0xfffffc00u);
  return f32x4{hi, lse - hi, 0.f, 0.f};
}

template <int N, int SLEEP, int BACK>
__device__ __forceinline__ void gather(u32x4 (&v)[N], __amdgpu_buffer_rsrc_t rws, unsigned (&off)[N], const unsigned (&so)[N], const Tag& tg) {
#pragma unroll
  for (int n = 0; n < N; ++n) v[n] = u32x4{0u, 0u, 0u, 0u};
#pragma unroll 1
  for (unsigned spins = 0; spins < SPIN_CAP; ++spins) {
    CBAR();
    u32x4 t[N];
#pragma unroll
    for (int n = 0; n < N; ++n) t[n] = __builtin_amdgcn_raw_buffer_load_b128(rws, off[n], so[n], 16);
    int left = 0;
#pragma unroll
    for (int n = 0; n < N; ++n) {
      const bool want = off[n] != OOB;
      if (want) v[n] = t[n];
      const bool miss = want && (tag_bad(t[n], tg) & 1023u) != 0;
      off[n] = miss ? off[n] : OOB;
      left += __builtin_popcountll(__builtin_amdgcn_ballot_w64(miss));
    }
    if (left == 0) break;
    if (SLEEP) __builtin_amdgcn_s_sleep(SLEEP);
    if (BACK) for (int i = left >> 4; i > 0; --i) __builtin_amdgcn_s_sleep(BACK);
  }
}

__global__ void __launch_bounds__(512, 2)
mha_fused(const char* __restrict__ x, const char* __restrict__ kc, const char* __restrict__ vc,
          const char* __restrict__ wq, const char* __restrict__ wo, char* __restrict__ out, char* __restrict__ ws) {
  extern __shared__ char smem_raw[];
  LAS char* smem = (LAS char*)smem_raw;
  const int lane = threadIdx.x & 63;
  const int wave = __builtin_amdgcn_readfirstlane(threadIdx.x >> 6);
  const int blk = blockIdx.x;
  const int h = (blk & 7) * 4 + (blk >> 6), j = (blk >> 3) & 7;
  const int par = (j >> 1) & 1;
  STAMP(0);

  if (wave < 4) {
    Stream s;
    s.rk = mkrsrc(kc, KV_BYTES); s.rv = mkrsrc(vc, KV_BYTES); s.voff = lane * 16;
    const unsigned base = ((unsigned)h << 19) | ((unsigned)(j & 5) << 16) | ((unsigned)par << 24) | ((unsigned)wave << 12);
    u32x4 ring[32];
    {
      u32x4 buf[64 + WIN];
#pragma unroll
      for (int n = 0; n < WIN; ++n) buf[n] = kv_load(s, n, base);
      SCHED();
      __builtin_amdgcn_s_barrier();
      SCHED();
#pragma unroll
      for (int n = 0; n < AHEAD; ++n) {
        asm volatile("" :: "v"(buf[n]));
        buf[n + WIN] = kv_load(s, (n + WIN) & 31, base + unit_off((n + WIN) >> 5));
        SCHED();
      }
      STAMP(1);
      lds_wait_ge(smem, C_Q, 1);
      STAMP(2);
      att_batch<0>(s, buf, base, 0, 0, smem, wave, lane);
      STAMP(3);
      att_batch<1>(s, buf, base, 0, 1, smem, wave, lane);
      STAMP(4);
#pragma unroll
      for (int n = 0; n < WIN; ++n) ring[n] = buf[64 + n];
    }
    lds_wait_ge(smem, C_Q, 2);
    for (int u = 2; u < 15; ++u) {
      att_batch<2>(s, ring, base + unit_off(u), base + unit_off(u + 1), u, smem, wave, lane);
      STAMP(3 + u);
    }
    att_batch<3>(s, ring, base + unit_off(15), 0, 15, smem, wave, lane);
    STAMP(18);
  } else {
    const int sw = wave - 4;
    const int c = lane & 15, g = lane >> 4, half = lane >> 5;
    const __amdgpu_buffer_rsrc_t rws = mkrsrc(ws, WS_SPAN);
    Tag tg;
    unsigned e1;
    {
      const __amdgpu_buffer_rsrc_t rq = mkrsrc(wq, DM * HD * 2), rx = mkrsrc(x, NB * DM * 2);
      const int row0 = 512 * (j >> 2) + 128 * sw;
      u32x4 wb[8], xf[4];
#pragma unroll
      for (int i = 0; i < 8; ++i)
        wb[i] = __builtin_amdgcn_raw_buffer_load_b128(rq, (lane >> 2) * (HD * 2) + (lane & 3) * 16, (row0 + 16 * i) * (HD * 2) + h * 256 + (j & 3) * 64, 2);
#pragma unroll
      for (int k = 0; k < 4; ++k) xf[k] = __builtin_amdgcn_raw_buffer_load_b128(rx, c * (DM * 2) + g * 16 + k * 64, row0 * 2, 0);
      const unsigned ep = __builtin_amdgcn_raw_buffer_load_b32(rws, 0, 0, 16);
      SCHED();
      if (sw == 0) {
        if (lane < 8) *lds_at<volatile unsigned>(smem, L_CNT + lane * 4) = 0u;
        *lds_at<volatile u32x4>(smem, L_HDR + lane * 16) = u32x4{0u, 0u, 0u, 0u};
        asm volatile("s_waitcnt lgkmcnt(0)" ::: "memory");
      }
      __builtin_amdgcn_s_barrier();
      SCHED();
#pragma unroll
      for (int i = 0; i < 8; ++i) *lds_at<volatile u32x4>(smem, L_WQ + sw * 8192 + i * 1024 + lane * 16) = wb[i];
      CBAR();
      f32x4 acc[2] = {f32x4{0.f, 0.f, 0.f, 0.f}, f32x4{0.f, 0.f, 0.f, 0.f}};
#pragma unroll
      for (int k = 0; k < 4; ++k)
#pragma unroll
        for (int t = 0; t < 2; ++t) {
          const int ad = L_WQ + sw * 8192 + (32 * k + 8 * g + (c >> 2)) * 64 + t * 32 + (c & 3) * 8;
          const s16x4 lo = __builtin_amdgcn_ds_read_tr16_b64_v4i16(lds_at<s16x4>(smem, ad));
          const s16x4 hi = __builtin_amdgcn_ds_read_tr16_b64_v4i16(lds_at<s16x4>(smem, ad + 256));
          const s16x8 fr = __builtin_shufflevector(lo, hi, 0, 1, 2, 3, 4, 5, 6, 7);
          acc[t] = __builtin_amdgcn_mfma_f32_16x16x32_bf16(__builtin_bit_cast(bf16x8, fr), __builtin_bit_cast(bf16x8, xf[k]), acc[t], 0, 0, 0);
        }
      *lds_at<volatile f32x4>(smem, L_QP + sw * 2048 + lane * 16) = acc[0];
      *lds_at<volatile f32x4>(smem, L_QP + sw * 2048 + 1024 + lane * 16) = acc[1];
      e1 = make_tag(ep, tg);
      lds_inc(smem, C_QP, lane);
      STAMP(1);
      if (sw < 2) {
        lds_wait_ge(smem, C_QP, 4);
        f32x4 sum = {0.f, 0.f, 0.f, 0.f};
#pragma unroll
        for (int w = 0; w < 4; ++w) sum += *lds_at<volatile f32x4>(smem, L_QP + w * 2048 + ((lane & 7) * 16 + 8 * sw + (lane >> 3)) * 16);
        __builtin_amdgcn_raw_buffer_store_b128(tag_pack(sum, tg), rws, lane * 16, WS_Q + (h * 8 + j) * Q_STRIDE + sw * 1024, 16);
      }
    }
    const unsigned grp = WS_PART + h * 8 * PART_STRIDE;
    if (sw == 0) {
      {
        const unsigned qo = (lane >> 3) * Q_STRIDE + (lane & 7) * 16, qs = WS_Q + h * 8 * Q_STRIDE + par * 128;
        {
          u32x4 v[1]; unsigned off[1] = {qo}; const unsigned so[1] = {qs};
          gather<1, QSLEEP, 0>(v, rws, off, so, tg);
          const f32x4 f = untag(v[0]);
          *lds_at<volatile unsigned>(smem, L_Q + (lane & 31) * 8 + half * 4) = pack_bf16(fold32(f[0], f[2]), fold32(f[1], f[3]));
          lds_inc(smem, C_Q, lane);
        }
        STAMP(2);
        u32x4 v[7]; unsigned off[7], so[7];
#pragma unroll
        for (int n = 0; n < 7; ++n) { off[n] = qo; so[n] = qs + (n + 1) * 256; }
        gather<7, QSLEEP, 0>(v, rws, off, so, tg);
#pragma unroll
        for (int n = 0; n < 7; ++n) {
          const f32x4 f = untag(v[n]);
          *lds_at<volatile unsigned>(smem, L_Q + (n + 1) * 256 + (lane & 31) * 8 + half * 4) = pack_bf16(fold32(f[0], f[2]), fold32(f[1], f[3]));
        }
        lds_inc(smem, C_Q, lane);
        STAMP(3);
      }
      u32x4 msg[4]; f32x4 lch = {0.f, 0.f, 0.f, 0.f};
#pragma unroll
      for (int i = 0; i < 4; ++i) {
        f32x4 o; float lse;
        merge8<16>(smem, i < 3 ? 2 * i + half : 6, lane, o, lse);
        msg[i] = tag_pack(o, tg);
        if ((lane & 31) == i) lch = lse_chunk(lse);
        asm volatile("" : "+v"(msg[i]), "+v"(lch));
      }
      const unsigned mine = grp + (par * 4 + (j & 1) + 2 * (j >> 2)) * PART_STRIDE;
      lds_wait_flags<16>(smem, 14, lane);
      lds_wait_ge(smem, C_TRIG, 4);
#pragma unroll
      for (int i = 0; i < 4; ++i)
        __builtin_amdgcn_raw_buffer_store_b128(msg[i], rws, (i < 3 || lane < 32) ? lane * 16u : OOB, mine + i * 1024, 16);
      __builtin_amdgcn_raw_buffer_store_b128(tag_pack(lch, tg), rws, (lane & 31) < 4 ? ((lane & 31) * 2 + half) * 16u : OOB, mine + 4096, 16);
      STAMP(17);
      {
        f32x4 o; float lse;
        merge8<0>(smem, 7, lane, o, lse);
        if (lane >= 32) o = lse_chunk(lse);
        __builtin_amdgcn_raw_buffer_store_b128(tag_pack(o, tg), rws, lane < 32 ? lane * 16u : lane < 40 ? lane * 16u + 128u : OOB, mine + 7 * 512, 16);
      }
      STAMP(18);
      lds_wait_ge(smem, C_O, 3);
      u32x4 v[8]; unsigned off[8], so[8];
#pragma unroll
      for (int n = 0; n < 8; ++n) { off[n] = g * P_STRIDE + c * 16; so[n] = WS_P + (j * NH + 4 * n) * P_STRIDE + h * 256; }
      gather<8, FSLEEP, FBACK>(v, rws, off, so, tg);
      f32x4 sum = untag(v[0]);
#pragma unroll
      for (int n = 1; n < 8; ++n) sum += untag(v[n]);
      const float y = fold32(fold16(sum[0], sum[1]), fold16(sum[2], sum[3]));
      auto sw16 = __builtin_amdgcn_permlane16_swap(asu(y), asu(y), false, false);
      const unsigned pk = pack_bf16(asf(sw16[0]), asf(sw16[1]));
      if ((g & 1) == 0) *(unsigned*)(out + c * (DM * 2) + (j * 128 + 32 * (h & 3) + 4 * (h >> 2) + g) * 2) = pk;
      if (blk == 0 && lane == 0) __builtin_amdgcn_raw_buffer_store_b32(e1, rws, 0, 0, 16);
      STAMP(20);
      if (blk == PBLK) {
        auto st = [&](int w, int sl) { return (unsigned)__builtin_amdgcn_readfirstlane((int)*lds_at<volatile unsigned>(smem, L_END + (w * 32 + sl) * 4)); };
        unsigned X = st(PW1, PS1) - st(PW0, PS0);
        if (X > 30000u) X = 30000u;
        const unsigned t0_ = (unsigned)__builtin_amdgcn_s_memrealtime();
#pragma unroll 1
        for (unsigned i = 0; i < (1u << 20); ++i) { if ((unsigned)__builtin_amdgcn_s_memrealtime() - t0_ >= X * PM) break; __builtin_amdgcn_s_sleep(1); }
      }
    } else if (sw == 1) {
      const __amdgpu_buffer_rsrc_t ro = mkrsrc(wo, HD * DM * 2);
      bf16x8 af[4][8];
      lds_wait_flags<16>(smem, WO_AT, lane);
#pragma unroll
      for (int k = 0; k < 4; ++k) {
        u32x4 wb[8];
#pragma unroll
        for (int i = 0; i < 8; ++i)
          wb[i] = __builtin_amdgcn_raw_buffer_load_b128(ro, g * (8 * DM * 2) + c * 16, (h * DH + k * 32 + i) * (DM * 2) + j * 256, 2);
#pragma unroll
        for (int t = 0; t < 8; ++t) { af[k][t] = colfrag(wb, t); asm volatile("" : "+v"(af[k][t])); }
        SCHED();
      }
      lds_wait_flags<16>(smem, 14, lane);
      lds_wait_flags<1>(smem, 15, lane);
      u32x4 v[5]; unsigned off[5], so[5];
#pragma unroll
      for (int i = 0; i < 4; ++i) { off[i] = half * 4 * PART_STRIDE + (lane & 31) * 16; so[i] = grp + i * PART_STRIDE + 7 * 512; }
      off[4] = (half * 4 + (lane & 3)) * PART_STRIDE; so[4] = grp + 4096 + 128;
      gather<5, G1SLEEP, 0>(v, rws, off, so, tg);
      const f32x4 lf = untag(v[4]);
      float M = fmaxf(lf[0], dpp<QX1>(lf[0])); M = fmaxf(M, dpp<QX2>(M));
      const float e = __builtin_amdgcn_exp2f((lf[0] - M) + lf[1]);
      float S = e + dpp<QX1>(e); S += dpp<QX2>(S);
      const float en = e * __builtin_amdgcn_rcpf(S);
      const f32x4 o = untag(v[0]) * dpp<0x00>(en) + untag(v[1]) * dpp<0x55>(en) + untag(v[2]) * dpp<0xAA>(en) + untag(v[3]) * dpp<0xFF>(en);
      *lds_at<volatile u32x2>(smem, L_O + (14 + half) * O_ROW + (lane & 31) * 8) = u32x2{pack_bf16(o[0], o[1]), pack_bf16(o[2], o[3])};
      STAMP(18);
      lds_wait_ge(smem, C_O, 2);
      STAMP(19);
      u32x4 of[4];
#pragma unroll
      for (int k = 0; k < 4; ++k) of[k] = *lds_at<volatile u32x4>(smem, L_O + c * O_ROW + k * 64 + g * 16);
      SCHED();
      f32x4 acc[8];
#pragma unroll
      for (int k = 0; k < 4; ++k)
#pragma unroll
        for (int t = 0; t < 8; ++t)
          acc[t] = __builtin_amdgcn_mfma_f32_16x16x32_bf16(af[k][t], __builtin_bit_cast(bf16x8, of[k]), k == 0 ? f32x4{0.f, 0.f, 0.f, 0.f} : acc[t], 0, 0, 0);
#pragma unroll
      for (int n = 0; n < 8; ++n) {
        const int i = n >> 1, th = n & 1;
        const f32x4 pv = {acc[4 * th][i], acc[4 * th + 1][i], acc[4 * th + 2][i], acc[4 * th + 3][i]};
        __builtin_amdgcn_raw_buffer_store_b128(tag_pack(pv, tg), rws, lane * 16, WS_P + (j * NH + h) * P_STRIDE + n * 1024, 16);
      }
      lds_inc(smem, C_O, lane);
      STAMP(20);
    } else {
      const int p0 = (sw - 2) * 4;
      lds_wait_flags<16>(smem, 14, lane);
      lds_wait_ge(smem, C_TRIG, 4);
      u32x4 lv[1];
      {
        unsigned off[1] = {(half * 4 + (lane & 3)) * PART_STRIDE + ((lane >> 2) & 3) * 16}; const unsigned so[1] = {grp + 4096 + p0 * 16};
        gather<1, G2SLEEP, 0>(lv, rws, off, so, tg);
      }
      STAMP(17);
      u32x4 v[16]; unsigned off[16], so[16];
#pragma unroll
      for (int n = 0; n < 16; ++n) {
        off[n] = (n >= 12 && sw == 3) ? OOB : half * 4 * PART_STRIDE + (lane & 31) * 16;
        so[n] = grp + (n & 3) * PART_STRIDE + (p0 + (n >> 2)) * 512;
      }
      gather<16, 2, 0>(v, rws, off, so, tg);
      const f32x4 lf = untag(lv[0]);
      float M = fmaxf(lf[0], dpp<QX1>(lf[0])); M = fmaxf(M, dpp<QX2>(M));
      const float e = __builtin_amdgcn_exp2f((lf[0] - M) + lf[1]);
      float S = e + dpp<QX1>(e); S += dpp<QX2>(S);
      const float en = e * __builtin_amdgcn_rcpf(S);
#pragma unroll
      for (int p = 0; p < 4; ++p) {
        f32x4 o = untag(v[4 * p]) * bcast16(en, 4 * p);
#pragma unroll
        for (int m = 1; m < 4; ++m) o += untag(v[4 * p + m]) * bcast16(en, 4 * p + m);
        if (!(p == 3 && sw == 3))
          *lds_at<volatile u32x2>(smem, L_O + (2 * (p0 + p) + half) * O_ROW + (lane & 31) * 8) = u32x2{pack_bf16(o[0], o[1]), pack_bf16(o[2], o[3])};
      }
      lds_inc(smem, C_O, lane);
      STAMP(18);
    }
  }
}

extern "C" void kernel_launch(void* const* d_in, const int* in_sizes, int n_in, void* d_out, int out_size, void* d_ws, size_t ws_size, hipStream_t stream) {
  (void)hipFuncSetAttribute((const void*)mha_fused, hipFuncAttributeMaxDynamicSharedMemorySize, LDS_BYTES);
  mha_fused<<<256, 512, LDS_BYTES, stream>>>((const char*)d_in[0], (const char*)d_in[1], (const char*)d_in[2],
                                             (const char*)d_in[3], (const char*)d_in[4], (char*)d_out, (char*)d_ws);
}
```

```cpp
#define PROBE2 1
#define PBLK 100
#define PM 0
#include <hip/hip_runtime.h>
#include <stdint.h>

typedef unsigned u32x4 __attribute__((ext_vector_type(4)));
typedef unsigned u32x2 __attribute__((ext_vector_type(2)));
typedef float f32x4 __attribute__((ext_vector_type(4)));
typedef float f32x2 __attribute__((ext_vector_type(2)));
typedef __bf16 bf16x2 __attribute__((ext_vector_type(2)));
typedef __bf16 bf16x8 __attribute__((ext_vector_type(8)));
typedef short s16x4 __attribute__((ext_vector_type(4)));
typedef short s16x8 __attribute__((ext_vector_type(8)));
#define LAS __attribute__((address_space(3)))

#define WIN 5
#define AHEAD 48
#define CATCH 4
#define QWIN 2
#define WO_AT 2
#define TRIG (31 - WIN)
#define KAUX 2
#define VAUX 2
#define QSLEEP 4
#define G1SLEEP 2
#define G2SLEEP 8
#define FBACK 1
#define FSLEEP 2

constexpr int NB = 16, DM = 1024, NH = 32, SK = 2048, DH = 128, HD = NH * DH;
constexpr unsigned KV_BYTES = 1u << 28;
constexpr unsigned WS_Q = 4096, Q_STRIDE = 2048;
constexpr unsigned WS_PART = 1u << 20, PART_STRIDE = 4096 + 256;
constexpr unsigned WS_P = 4u << 20, P_STRIDE = 8192;
constexpr unsigned WS_SPAN = 8u << 20;
constexpr unsigned OOB = 0x80000000u;
constexpr unsigned WS_PROF = 16u << 20;
constexpr int L_WQ = 0;
constexpr int L_QP = L_WQ + 4 * 8192;
constexpr int L_Q = L_QP + 4 * 2048;
constexpr int L_ACC = L_Q + 8 * 256;
constexpr int L_HDR = L_ACC + 16 * 4 * 512;
constexpr int O_ROW = 272;
constexpr int L_O = L_HDR + 16 * 4 * 16;
constexpr int L_CNT = L_O + 16 * O_ROW;
constexpr int C_QP = L_CNT, C_Q = L_CNT + 4, C_TRIG = L_CNT + 8, C_O = L_CNT + 12, C_EP = L_CNT + 16;
constexpr int L_END = L_CNT + 64;
constexpr int LDS_BYTES = 96 * 1024;
static_assert(L_END + 1024 <= LDS_BYTES, "lds");
constexpr unsigned SPIN_CAP = 1u << 15;

__device__ __forceinline__ float asf(unsigned u) { return __builtin_bit_cast(float, u); }
__device__ __forceinline__ unsigned asu(float f) { return __builtin_bit_cast(unsigned, f); }
__device__ __forceinline__ float dot2(unsigned a, unsigned b, float acc) {
  return __builtin_amdgcn_fdot2_f32_bf16(__builtin_bit_cast(bf16x2, a), __builtin_bit_cast(bf16x2, b), acc, false);
}
template <int CTRL> __device__ __forceinline__ float dpp(float x) {
  return asf((unsigned)__builtin_amdgcn_mov_dpp((int)asu(x), CTRL, 0xf, 0xf, true));
}
constexpr int QX1 = 0xB1, QX2 = 0x4E, HMIR = 0x141, ROR4 = 0x124, ROR8 = 0x128, BCAST = 0x150;
__device__ __forceinline__ unsigned pack_bf16(float lo, float hi) {
  bf16x2 r; r[0] = (__bf16)lo; r[1] = (__bf16)hi; return __builtin_bit_cast(unsigned, r);
}
__device__ __forceinline__ float fold16(float a, float b) {
  auto r = __builtin_amdgcn_permlane16_swap(asu(a), asu(b), false, false); return asf(r[0]) + asf(r[1]);
}
__device__ __forceinline__ float fold32(float a, float b) {
  auto r = __builtin_amdgcn_permlane32_swap(asu(a), asu(b), false, false); return asf(r[0]) + asf(r[1]);
}
__device__ __forceinline__ float max16(float a) {
  auto r = __builtin_amdgcn_permlane16_swap(asu(a), asu(a), false, false); return fmaxf(asf(r[0]), asf(r[1]));
}
__device__ __forceinline__ float max32(float a) {
  auto r = __builtin_amdgcn_permlane32_swap(asu(a), asu(a), false, false); return fmaxf(asf(r[0]), asf(r[1]));
}
__device__ __forceinline__ __amdgpu_buffer_rsrc_t mkrsrc(const void* p, unsigned bytes) {
  return __builtin_amdgcn_make_buffer_rsrc((void*)p, 0, (int)bytes, 0x00020000);
}
template <typename T> __device__ __forceinline__ LAS T* lds_at(LAS char* s, int off) { return (LAS T*)(s + off); }
#define CBAR() asm volatile("" ::: "memory")
#define SCHED() __builtin_amdgcn_sched_barrier(0)

#define STAMP(slot) do { unsigned t_ = (unsigned)__builtin_amdgcn_s_memrealtime(); if (lane == 0) *lds_at<volatile unsigned>(smem, L_END + (wave * 32 + (slot)) * 4) = t_; } while (0)

__device__ __forceinline__ void lds_wait_ge(LAS char* smem, int off, unsigned want) {
#pragma unroll 1
  for (unsigned spins = 0; spins < SPIN_CAP; ++spins) {
    unsigned v = *lds_at<volatile unsigned>(smem, off);
    if ((unsigned)__builtin_amdgcn_readfirstlane((int)v) >= want) break;
    __builtin_amdgcn_s_sleep(1);
  }
  CBAR();
}

template <int SLEEP>
__device__ __forceinline__ void lds_wait_flags(LAS char* smem, int u, int lane) {
#pragma unroll 1
  for (unsigned spins = 0; spins < SPIN_CAP; ++spins) {
    const unsigned f = *lds_at<volatile unsigned>(smem, L_HDR + (u * 4 + (lane & 3)) * 16 + 8);
    if (__builtin_amdgcn_ballot_w64(f != 1u) == 0) break;
    __builtin_amdgcn_s_sleep(SLEEP);
  }
  CBAR();
}

__device__ __forceinline__ bf16x8 colfrag(const u32x4* r, int t) {
  u32x4 f;
#pragma unroll
  for (int u = 0; u < 4; ++u)
    f[u] = __builtin_amdgcn_perm(r[2 * u + 1][t >> 1], r[2 * u][t >> 1], (t & 1) ? 0x07060302u : 0x05040100u);
  return __builtin_bit_cast(bf16x8, f);
}

struct Tag { unsigned t[4]; };
__device__ __forceinline__ u32x4 tag_pack(f32x4 v, const Tag& g) {
  u32x4 r;
#pragma unroll
  for (int i = 0; i < 4; ++i) r[i] = (asu(v[i]) & 0xfffffc00u) | g.t[i];
  return r;
}
__device__ __forceinline__ unsigned tag_bad(u32x4 v, const Tag& g) {
  return ((v[0] ^ g.t[0]) | (v[1] ^ g.t[1]) | (v[2] ^ g.t[2]) | (v[3] ^ g.t[3]));
}
__device__ __forceinline__ f32x4 untag(u32x4 v) {
  f32x4 r;
#pragma unroll
  for (int i = 0; i < 4; ++i) r[i] = asf(v[i] & 0xfffffc00u);
  return r;
}

__device__ __forceinline__ unsigned make_tag(unsigned ep, Tag& tg) {
  const unsigned e1 = (unsigned)__builtin_amdgcn_readfirstlane((int)ep) + 1u;
  tg.t[0] = e1 & 1023u; tg.t[1] = (e1 + (e1 >> 10)) & 1023u; tg.t[2] = (e1 + (e1 >> 20)) & 1023u; tg.t[3] = (e1 + (e1 >> 30) + 341u) & 1023u;
  asm volatile("" :: "s"(e1) : "memory");
  return e1;
}
__device__ __forceinline__ float bcast16(float x, int l) {
  switch (l) {
    case 0: return dpp<BCAST + 0>(x); case 1: return dpp<BCAST + 1>(x); case 2: return dpp<BCAST + 2>(x); case 3: return dpp<BCAST + 3>(x);
    case 4: return dpp<BCAST + 4>(x); case 5: return dpp<BCAST + 5>(x); case 6: return dpp<BCAST + 6>(x); case 7: return dpp<BCAST + 7>(x);
    case 8: return dpp<BCAST + 8>(x); case 9: return dpp<BCAST + 9>(x); case 10: return dpp<BCAST + 10>(x); case 11: return dpp<BCAST + 11>(x);
    case 12: return dpp<BCAST + 12>(x); case 13: return dpp<BCAST + 13>(x); case 14: return dpp<BCAST + 14>(x); default: return dpp<BCAST + 15>(x);
  }
}

struct Stream { __amdgpu_buffer_rsrc_t rk, rv; unsigned voff; };
__device__ __forceinline__ constexpr unsigned unit_off(int u) { return ((unsigned)(u & 1) << 17) | ((unsigned)(u >> 1) << 25); }
__device__ __forceinline__ u32x4 kv_load(const Stream& s, int n, unsigned sb) {
  const int i = n & 15;
  const unsigned so = sb + (unsigned)(((i >> 2) & 1) << 15) + (unsigned)(((i >> 3) & 1) << 14);
  const unsigned vo = s.voff + (unsigned)((i & 3) << 10);
  return (n & 16) ? __builtin_amdgcn_raw_buffer_load_b128(s.rv, vo, so, VAUX) : __builtin_amdgcn_raw_buffer_load_b128(s.rk, vo, so, KAUX);
}

constexpr int issue_upto(int n) {
  return (n + WIN + 1 > AHEAD + WIN + n / CATCH) ? n + WIN + 1 : AHEAD + WIN + n / CATCH;
}
static_assert(issue_upto(63) == 64 + WIN, "catch-up must end within two batches");

__device__ __forceinline__ void lds_inc(LAS char* smem, int off, int lane) {
  CBAR();
  if (lane == 0) __hip_atomic_fetch_add(lds_at<unsigned>(smem, off), 1u, __ATOMIC_RELAXED, __HIP_MEMORY_SCOPE_WORKGROUP);
  CBAR();
}

template <int MODE, int N>
__device__ __forceinline__ void att_batch(const Stream& s, u32x4 (&buf)[N], unsigned sb, unsigned sbn, int b, LAS char* smem, int wave, int lane) {
  constexpr float C2 = 0.088388347648318440f * 1.4426950408889634f;
  const int c = lane & 15, r = lane >> 4;
  const u32x4 qv = *lds_at<u32x4>(smem, L_Q + (b >> 1) * 256 + c * 16);
  const bool b3 = (lane & 8) != 0, b2 = (lane & 4) != 0;
  float a[2], u[2], v[4], p[4];
  f32x2 acc[4];
  float mneg = 0.f;
#pragma unroll
  for (int n = 0; n < 32; ++n) {
    constexpr int OFF = MODE == 1 ? 32 : 0;
    if (MODE >= 2) {
      asm volatile("" :: "v"(buf[n]));
      if (MODE == 2 || n + WIN < 32) buf[(n + WIN) & 31] = kv_load(s, (n + WIN) & 31, n + WIN < 32 ? sb : sbn);
      if (MODE == 3 && n == TRIG) lds_inc(smem, C_TRIG, lane);
    } else {
      const int g = n + OFF;
#pragma unroll
      for (int i = (g == 0 ? AHEAD + WIN : issue_upto(g - 1)); i < issue_upto(g); ++i) {
        asm volatile("" :: "v"(buf[i - WIN]));
        buf[i] = kv_load(s, i & 31, sb + unit_off(i >> 5));
      }
    }
    SCHED();
    const u32x4 d = buf[n + OFF];
    if (n < 16) {
      float t = dot2(d[0], qv[0], 0.f);
      t = dot2(d[1], qv[1], t); t = dot2(d[2], qv[2], t); t = dot2(d[3], qv[3], t);
      a[n & 1] = t;
      if ((n & 1) == 1) {
        const float keep = b3 ? a[1] : a[0], send = b3 ? a[0] : a[1];
        u[(n >> 1) & 1] = keep + dpp<ROR8>(send);
      }
      if ((n & 3) == 3) {
        const float keep = b2 ? u[1] : u[0], send = b2 ? u[0] : u[1];
        float x = keep + dpp<HMIR>(send);
        x += dpp<QX1>(x); x += dpp<QX2>(x);
        v[n >> 2] = x;
      }
      if (n == 15) {
        float m = fmaxf(fmaxf(v[0], v[1]), fmaxf(v[2], v[3]));
        m = fmaxf(m, dpp<ROR4>(m)); m = fmaxf(m, dpp<ROR8>(m));
        m = max16(m); m = max32(m);
        mneg = -m * C2;
#pragma unroll
        for (int k = 0; k < 4; ++k) p[k] = __builtin_amdgcn_exp2f(fmaf(v[k], C2, mneg));
      }
    } else {
      const int i = n - 16;
      float pb;
      switch (i & 3) {
        case 0: pb = dpp<BCAST + 0>(p[i >> 2]); break;
        case 1: pb = dpp<BCAST + 8>(p[i >> 2]); break;
        case 2: pb = dpp<BCAST + 4>(p[i >> 2]); break;
        default: pb = dpp<BCAST + 12>(p[i >> 2]); break;
      }
      const f32x2 pp = {pb, pb};
#pragma unroll
      for (int j = 0; j < 4; ++j) {
        const f32x2 x = {asf(d[j] << 16), asf(d[j] & 0xffff0000u)};
        acc[j] = (i == 0) ? pp * x : __builtin_elementwise_fma(pp, x, acc[j]);
      }
    }
    SCHED();
  }
  float l = (p[0] + p[1]) + (p[2] + p[3]);
  l += dpp<ROR4>(l); l += dpp<ROR8>(l);
  l = fold16(l, l); l = fold32(l, l);
  f32x2 o;
  o[0] = fold32(fold16(acc[0][0], acc[1][0]), fold16(acc[2][0], acc[3][0]));
  o[1] = fold32(fold16(acc[0][1], acc[1][1]), fold16(acc[2][1], acc[3][1]));
  *lds_at<volatile f32x2>(smem, L_ACC + (b * 4 + wave) * 512 + c * 32 + r * 8) = o;
  CBAR();
  if (lane == 0) {
    u32x4 h = {asu(-mneg), asu(l), 1u, 0u};
    *lds_at<volatile u32x4>(smem, L_HDR + (b * 4 + wave) * 16) = h;
  }
  CBAR();
}

template <int SLEEP>
__device__ __forceinline__ void merge8(LAS char* smem, int pp, int lane, f32x4& o, float& lse) {
  u32x4 hd; f32x4 pa[8];
  if (SLEEP) lds_wait_flags<SLEEP>(smem, 2 * pp + 1, lane);
#pragma unroll 1
  for (unsigned spins = 0; spins < SPIN_CAP; ++spins) {
    hd = *lds_at<volatile u32x4>(smem, L_HDR + (pp * 8 + (lane & 7)) * 16);
#pragma unroll
    for (int w = 0; w < 8; ++w) pa[w] = *lds_at<volatile f32x4>(smem, L_ACC + (pp * 8 + w) * 512 + (lane & 31) * 16);
    if (__builtin_amdgcn_ballot_w64(hd[2] != 1u) == 0) break;
  }
  const float m = asf(hd[0]);
  float M = fmaxf(m, dpp<QX1>(m)); M = fmaxf(M, dpp<QX2>(M)); M = fmaxf(M, dpp<HMIR>(M));
  const float e = __builtin_amdgcn_exp2f(m - M);
  float L = e * asf(hd[1]); L += dpp<QX1>(L); L += dpp<QX2>(L); L += dpp<HMIR>(L);
  const float en = e * __builtin_amdgcn_rcpf(L);
  o = pa[0] * bcast16(en, 0);
#pragma unroll
  for (int w = 1; w < 8; ++w) o += pa[w] * bcast16(en, w);
  lse = M + __builtin_amdgcn_logf(L);
}
__device__ __forceinline__ f32x4 lse_chunk(float lse) {
  const float hi = asf(asu(lse) & 0xfffffc00u);
  return f32x4{hi, lse - hi, 0.f, 0.f};
}

template <int N, int SLEEP, int BACK>
__device__ __forceinline__ void gather(u32x4 (&v)[N], __amdgpu_buffer_rsrc_t rws, unsigned (&off)[N], const unsigned (&so)[N], const Tag& tg) {
#pragma unroll
  for (int n = 0; n < N; ++n) v[n] = u32x4{0u, 0u, 0u, 0u};
#pragma unroll 1
  for (unsigned spins = 0; spins < SPIN_CAP; ++spins) {
    CBAR();
    u32x4 t[N];
#pragma unroll
    for (int n = 0; n < N; ++n) t[n] = __builtin_amdgcn_raw_buffer_load_b128(rws, off[n], so[n], 16);
    int left = 0;
#pragma unroll
    for (int n = 0; n < N; ++n) {
      const bool want = off[n] != OOB;
      if (want) v[n] = t[n];
      const bool miss = want && (tag_bad(t[n], tg) & 1023u) != 0;
      off[n] = miss ? off[n] : OOB;
      left += __builtin_popcountll(__builtin_amdgcn_ballot_w64(miss));
    }
    if (left == 0) break;
    if (SLEEP) __builtin_amdgcn_s_sleep(SLEEP);
    if (BACK) for (int i = left >> 4; i > 0; --i) __builtin_amdgcn_s_sleep(BACK);
  }
}

__global__ void __launch_bounds__(512, 2)
mha_fused(const char* __restrict__ x, const char* __restrict__ kc, const char* __restrict__ vc,
          const char* __restrict__ wq, const char* __restrict__ wo, char* __restrict__ out, char* __restrict__ ws) {
  extern __shared__ char smem_raw[];
  LAS char* smem = (LAS char*)smem_raw;
  const int lane = threadIdx.x & 63;
  const int wave = __builtin_amdgcn_readfirstlane(threadIdx.x >> 6);
  const int blk = blockIdx.x;
  const int h = (blk & 7) * 4 + (blk >> 6), j = (blk >> 3) & 7;
  const int par = (j >> 1) & 1;
  STAMP(0);

  if (wave < 4) {
    Stream s;
    s.rk = mkrsrc(kc, KV_BYTES); s.rv = mkrsrc(vc, KV_BYTES); s.voff = lane * 16;
    const unsigned base = ((unsigned)h << 19) | ((unsigned)(j & 5) << 16) | ((unsigned)par << 24) | ((unsigned)wave << 12);
    u32x4 ring[32];
    {
      u32x4 buf[64 + WIN];
#pragma unroll
      for (int n = 0; n < WIN; ++n) buf[n] = kv_load(s, n, base);
      SCHED();
      __builtin_amdgcn_s_barrier();
      SCHED();
#pragma unroll
      for (int n = 0; n < AHEAD; ++n) {
        asm volatile("" :: "v"(buf[n]));
        buf[n + WIN] = kv_load(s, (n + WIN) & 31, base + unit_off((n + WIN) >> 5));
        SCHED();
      }
      STAMP(1);
      lds_wait_ge(smem, C_Q, 1);
      STAMP(2);
      att_batch<0>(s, buf, base, 0, 0, smem, wave, lane);
      STAMP(3);
      att_batch<1>(s, buf, base, 0, 1, smem, wave, lane);
      STAMP(4);
#pragma unroll
      for (int n = 0; n < WIN; ++n) ring[n] = buf[64 + n];
    }
    lds_wait_ge(smem, C_Q, 2);
    for (int u = 2; u < 15; ++u) {
      att_batch<2>(s, ring, base + unit_off(u), base + unit_off(u + 1), u, smem, wave, lane);
      STAMP(3 + u);
    }
    att_batch<3>(s, ring, base + unit_off(15), 0, 15, smem, wave, lane);
    STAMP(18);
  } else {
    const int sw = wave - 4;
    const int c = lane & 15, g = lane >> 4, half = lane >> 5;
    const __amdgpu_buffer_rsrc_t rws = mkrsrc(ws, WS_SPAN);
    Tag tg;
    unsigned e1;
    unsigned prevEnd = 0;
    if (sw == 0) {
      const __amdgpu_buffer_rsrc_t rp = mkrsrc(ws + (32u << 20), 1u << 20);
      const unsigned my0 = (unsigned)__builtin_amdgcn_s_memrealtime();
      if (lane == 0) __builtin_amdgcn_raw_buffer_store_b32(my0, rp, blk * 128, 0, 16);
      if (blk == PBLK) { prevEnd = my0 - 50000u; for (int i = 0; i < 4; ++i) { const unsigned t = __builtin_amdgcn_raw_buffer_load_b32(rp, (i * 64 + lane) * 128 + 65536, 0, 16); if (my0 - t < my0 - prevEnd) prevEnd = t; } }
    }
    {
      const __amdgpu_buffer_rsrc_t rq = mkrsrc(wq, DM * HD * 2), rx = mkrsrc(x, NB * DM * 2);
      const int row0 = 512 * (j >> 2) + 128 * sw;
      u32x4 wb[8], xf[4];
#pragma unroll
      for (int i = 0; i < 8; ++i)
        wb[i] = __builtin_amdgcn_raw_buffer_load_b128(rq, (lane >> 2) * (HD * 2) + (lane & 3) * 16, (row0 + 16 * i) * (HD * 2) + h * 256 + (j & 3) * 64, 2);
#pragma unroll
      for (int k = 0; k < 4; ++k) xf[k] = __builtin_amdgcn_raw_buffer_load_b128(rx, c * (DM * 2) + g * 16 + k * 64, row0 * 2, 0);
      const unsigned ep = __builtin_amdgcn_raw_buffer_load_b32(rws, 0, 0, 16);
      SCHED();
      if (sw == 0) {
        if (lane < 8) *lds_at<volatile unsigned>(smem, L_CNT + lane * 4) = 0u;
        *lds_at<volatile u32x4>(smem, L_HDR + lane * 16) = u32x4{0u, 0u, 0u, 0u};
        asm volatile("s_waitcnt lgkmcnt(0)" ::: "memory");
      }
      __builtin_amdgcn_s_barrier();
      SCHED();
#pragma unroll
      for (int i = 0; i < 8; ++i) *lds_at<volatile u32x4>(smem, L_WQ + sw * 8192 + i * 1024 + lane * 16) = wb[i];
      CBAR();
      f32x4 acc[2] = {f32x4{0.f, 0.f, 0.f, 0.f}, f32x4{0.f, 0.f, 0.f, 0.f}};
#pragma unroll
      for (int k = 0; k < 4; ++k)
#pragma unroll
        for (int t = 0; t < 2; ++t) {
          const int ad = L_WQ + sw * 8192 + (32 * k + 8 * g + (c >> 2)) * 64 + t * 32 + (c & 3) * 8;
          const s16x4 lo = __builtin_amdgcn_ds_read_tr16_b64_v4i16(lds_at<s16x4>(smem, ad));
          const s16x4 hi = __builtin_amdgcn_ds_read_tr16_b64_v4i16(lds_at<s16x4>(smem, ad + 256));
          const s16x8 fr = __builtin_shufflevector(lo, hi, 0, 1, 2, 3, 4, 5, 6, 7);
          acc[t] = __builtin_amdgcn_mfma_f32_16x16x32_bf16(__builtin_bit_cast(bf16x8, fr), __builtin_bit_cast(bf16x8, xf[k]), acc[t], 0, 0, 0);
        }
      *lds_at<volatile f32x4>(smem, L_QP + sw * 2048 + lane * 16) = acc[0];
      *lds_at<volatile f32x4>(smem, L_QP + sw * 2048 + 1024 + lane * 16) = acc[1];
      e1 = make_tag(ep, tg);
      lds_inc(smem, C_QP, lane);
      STAMP(1);
      if (sw < 2) {
        lds_wait_ge(smem, C_QP, 4);
        f32x4 sum = {0.f, 0.f, 0.f, 0.f};
#pragma unroll
        for (int w = 0; w < 4; ++w) sum += *lds_at<volatile f32x4>(smem, L_QP + w * 2048 + ((lane & 7) * 16 + 8 * sw + (lane >> 3)) * 16);
        __builtin_amdgcn_raw_buffer_store_b128(tag_pack(sum, tg), rws, lane * 16, WS_Q + (h * 8 + j) * Q_STRIDE + sw * 1024, 16);
      }
    }
    const unsigned grp = WS_PART + h * 8 * PART_STRIDE;
    if (sw == 0) {
      {
        const unsigned qo = (lane >> 3) * Q_STRIDE + (lane & 7) * 16, qs = WS_Q + h * 8 * Q_STRIDE + par * 128;
        {
          u32x4 v[1]; unsigned off[1] = {qo}; const unsigned so[1] = {qs};
          gather<1, QSLEEP, 0>(v, rws, off, so, tg);
          const f32x4 f = untag(v[0]);
          *lds_at<volatile unsigned>(smem, L_Q + (lane & 31) * 8 + half * 4) = pack_bf16(fold32(f[0], f[2]), fold32(f[1], f[3]));
          lds_inc(smem, C_Q, lane);
        }
        STAMP(2);
        u32x4 v[7]; unsigned off[7], so[7];
#pragma unroll
        for (int n = 0; n < 7; ++n) { off[n] = qo; so[n] = qs + (n + 1) * 256; }
        gather<7, QSLEEP, 0>(v, rws, off, so, tg);
#pragma unroll
        for (int n = 0; n < 7; ++n) {
          const f32x4 f = untag(v[n]);
          *lds_at<volatile unsigned>(smem, L_Q + (n + 1) * 256 + (lane & 31) * 8 + half * 4) = pack_bf16(fold32(f[0], f[2]), fold32(f[1], f[3]));
        }
        lds_inc(smem, C_Q, lane);
        STAMP(3);
      }
      u32x4 msg[4]; f32x4 lch = {0.f, 0.f, 0.f, 0.f};
#pragma unroll
      for (int i = 0; i < 4; ++i) {
        f32x4 o; float lse;
        merge8<16>(smem, i < 3 ? 2 * i + half : 6, lane, o, lse);
        msg[i] = tag_pack(o, tg);
        if ((lane & 31) == i) lch = lse_chunk(lse);
        asm volatile("" : "+v"(msg[i]), "+v"(lch));
      }
      const unsigned mine = grp + (par * 4 + (j & 1) + 2 * (j >> 2)) * PART_STRIDE;
      lds_wait_flags<16>(smem, 14, lane);
      lds_wait_ge(smem, C_TRIG, 4);
#pragma unroll
      for (int i = 0; i < 4; ++i)
        __builtin_amdgcn_raw_buffer_store_b128(msg[i], rws, (i < 3 || lane < 32) ? lane * 16u : OOB, mine + i * 1024, 16);
      __builtin_amdgcn_raw_buffer_store_b128(tag_pack(lch, tg), rws, (lane & 31) < 4 ? ((lane & 31) * 2 + half) * 16u : OOB, mine + 4096, 16);
      STAMP(17);
      {
        f32x4 o; float lse;
        merge8<0>(smem, 7, lane, o, lse);
        if (lane >= 32) o = lse_chunk(lse);
        __builtin_amdgcn_raw_buffer_store_b128(tag_pack(o, tg), rws, lane < 32 ? lane * 16u : lane < 40 ? lane * 16u + 128u : OOB, mine + 7 * 512, 16);
      }
      STAMP(18);
      lds_wait_ge(smem, C_O, 3);
      u32x4 v[8]; unsigned off[8], so[8];
#pragma unroll
      for (int n = 0; n < 8; ++n) { off[n] = g * P_STRIDE + c * 16; so[n] = WS_P + (j * NH + 4 * n) * P_STRIDE + h * 256; }
      gather<8, FSLEEP, FBACK>(v, rws, off, so, tg);
      f32x4 sum = untag(v[0]);
#pragma unroll
      for (int n = 1; n < 8; ++n) sum += untag(v[n]);
      const float y = fold32(fold16(sum[0], sum[1]), fold16(sum[2], sum[3]));
      auto sw16 = __builtin_amdgcn_permlane16_swap(asu(y), asu(y), false, false);
      const unsigned pk = pack_bf16(asf(sw16[0]), asf(sw16[1]));
      if ((g & 1) == 0) *(unsigned*)(out + c * (DM * 2) + (j * 128 + 32 * (h & 3) + 4 * (h >> 2) + g) * 2) = pk;
      if (blk == 0 && lane == 0) __builtin_amdgcn_raw_buffer_store_b32(e1, rws, 0, 0, 16);
      STAMP(20);
      {
        const __amdgpu_buffer_rsrc_t rp = mkrsrc(ws + (32u << 20), 1u << 20);
        if (blk == PBLK) {
          unsigned mn = 0x7fffffffu, mx = 0u, pe = 0u;
          const unsigned my0 = (unsigned)__builtin_amdgcn_readfirstlane((int)*lds_at<volatile unsigned>(smem, L_END));
          for (int i = 0; i < 4; ++i) {
            const unsigned t = __builtin_amdgcn_raw_buffer_load_b32(rp, (i * 64 + lane) * 128, 0, 16) - my0 + 100000u;
            mn = t < mn ? t : mn; mx = t > mx ? t : mx;
          }
          pe = prevEnd - my0 + 100000u;
          for (int o = 32; o > 0; o >>= 1) { unsigned a = __shfl_xor(mn, o), b = __shfl_xor(mx, o), c2 = __shfl_xor(pe, o); mn = a < mn ? a : mn; mx = b > mx ? b : mx; pe = c2 > pe ? c2 : pe; }
          mn = __builtin_amdgcn_readfirstlane(mn); mx = __builtin_amdgcn_readfirstlane(mx); pe = __builtin_amdgcn_readfirstlane(pe);
          const unsigned t0_ = (unsigned)__builtin_amdgcn_s_memrealtime();
          unsigned X = PROBE2 == 1 ? mx - mn : PROBE2 == 2 ? t0_ - my0 + 100000u - mn : PROBE2 == 3 ? mn - pe : 100000u - mn;
          if (X > 10000u) X = 0u;
#pragma unroll 1
          for (unsigned i = 0; i < (1u << 20); ++i) { if ((unsigned)__builtin_amdgcn_s_memrealtime() - t0_ >= X * PM) break; __builtin_amdgcn_s_sleep(1); }
        }
        if (lane == 0) __builtin_amdgcn_raw_buffer_store_b32((unsigned)__builtin_amdgcn_s_memrealtime(), rp, blk * 128 + 65536, 0, 16);
      }
    } else if (sw == 1) {
      const __amdgpu_buffer_rsrc_t ro = mkrsrc(wo, HD * DM * 2);
      bf16x8 af[4][8];
      lds_wait_flags<16>(smem, WO_AT, lane);
#pragma unroll
      for (int k = 0; k < 4; ++k) {
        u32x4 wb[8];
#pragma unroll
        for (int i = 0; i < 8; ++i)
          wb[i] = __builtin_amdgcn_raw_buffer_load_b128(ro, g * (8 * DM * 2) + c * 16, (h * DH + k * 32 + i) * (DM * 2) + j * 256, 2);
#pragma unroll
        for (int t = 0; t < 8; ++t) { af[k][t] = colfrag(wb, t); asm volatile("" : "+v"(af[k][t])); }
        SCHED();
      }
      lds_wait_flags<16>(smem, 14, lane);
      lds_wait_flags<1>(smem, 15, lane);
      u32x4 v[5]; unsigned off[5], so[5];
#pragma unroll
      for (int i = 0; i < 4; ++i) { off[i] = half * 4 * PART_STRIDE + (lane & 31) * 16; so[i] = grp + i * PART_STRIDE + 7 * 512; }
      off[4] = (half * 4 + (lane & 3)) * PART_STRIDE; so[4] = grp + 4096 + 128;
      gather<5, G1SLEEP, 0>(v, rws, off, so, tg);
      const f32x4 lf = untag(v[4]);
      float M = fmaxf(lf[0], dpp<QX1>(lf[0])); M = fmaxf(M, dpp<QX2>(M));
      const float e = __builtin_amdgcn_exp2f((lf[0] - M) + lf[1]);
      float S = e + dpp<QX1>(e); S += dpp<QX2>(S);
      const float en = e * __builtin_amdgcn_rcpf(S);
      const f32x4 o = untag(v[0]) * dpp<0x00>(en) + untag(v[1]) * dpp<0x55>(en) + untag(v[2]) * dpp<0xAA>(en) + untag(v[3]) * dpp<0xFF>(en);
      *lds_at<volatile u32x2>(smem, L_O + (14 + half) * O_ROW + (lane & 31) * 8) = u32x2{pack_bf16(o[0], o[1]), pack_bf16(o[2], o[3])};
      STAMP(18);
      lds_wait_ge(smem, C_O, 2);
      STAMP(19);
      u32x4 of[4];
#pragma unroll
      for (int k = 0; k < 4; ++k) of[k] = *lds_at<volatile u32x4>(smem, L_O + c * O_ROW + k * 64 + g * 16);
      SCHED();
      f32x4 acc[8];
#pragma unroll
      for (int k = 0; k < 4; ++k)
#pragma unroll
        for (int t = 0; t < 8; ++t)
          acc[t] = __builtin_amdgcn_mfma_f32_16x16x32_bf16(af[k][t], __builtin_bit_cast(bf16x8, of[k]), k == 0 ? f32x4{0.f, 0.f, 0.f, 0.f} : acc[t], 0, 0, 0);
#pragma unroll
      for (int n = 0; n < 8; ++n) {
        const int i = n >> 1, th = n & 1;
        const f32x4 pv = {acc[4 * th][i], acc[4 * th + 1][i], acc[4 * th + 2][i], acc[4 * th + 3][i]};
        __builtin_amdgcn_raw_buffer_store_b128(tag_pack(pv, tg), rws, lane * 16, WS_P + (j * NH + h) * P_STRIDE + n * 1024, 16);
      }
      lds_inc(smem, C_O, lane);
      STAMP(20);
    } else {
      const int p0 = (sw - 2) * 4;
      lds_wait_flags<16>(smem, 14, lane);
      lds_wait_ge(smem, C_TRIG, 4);
      u32x4 lv[1];
      {
        unsigned off[1] = {(half * 4 + (lane & 3)) * PART_STRIDE + ((lane >> 2) & 3) * 16}; const unsigned so[1] = {grp + 4096 + p0 * 16};
        gather<1, G2SLEEP, 0>(lv, rws, off, so, tg);
      }
      STAMP(17);
      u32x4 v[16]; unsigned off[16], so[16];
#pragma unroll
      for (int n = 0; n < 16; ++n) {
        off[n] = (n >= 12 && sw == 3) ? OOB : half * 4 * PART_STRIDE + (lane & 31) * 16;
        so[n] = grp + (n & 3) * PART_STRIDE + (p0 + (n >> 2)) * 512;
      }
      gather<16, 2, 0>(v, rws, off, so, tg);
      const f32x4 lf = untag(lv[0]);
      float M = fmaxf(lf[0], dpp<QX1>(lf[0])); M = fmaxf(M, dpp<QX2>(M));
      const float e = __builtin_amdgcn_exp2f((lf[0] - M) + lf[1]);
      float S = e + dpp<QX1>(e); S += dpp<QX2>(S);
      const float en = e * __builtin_amdgcn_rcpf(S);
#pragma unroll
      for (int p = 0; p < 4; ++p) {
        f32x4 o = untag(v[4 * p]) * bcast16(en, 4 * p);
#pragma unroll
        for (int m = 1; m < 4; ++m) o += untag(v[4 * p + m]) * bcast16(en, 4 * p + m);
        if (!(p == 3 && sw == 3))
          *lds_at<volatile u32x2>(smem, L_O + (2 * (p0 + p) + half) * O_ROW + (lane & 31) * 8) = u32x2{pack_bf16(o[0], o[1]), pack_bf16(o[2], o[3])};
      }
      lds_inc(smem, C_O, lane);
      STAMP(18);
    }
  }
}

extern "C" void kernel_launch(void* const* d_in, const int* in_sizes, int n_in, void* d_out, int out_size, void* d_ws, size_t ws_size, hipStream_t stream) {
  (void)hipFuncSetAttribute((const void*)mha_fused, hipFuncAttributeMaxDynamicSharedMemorySize, LDS_BYTES);
  mha_fused<<<256, 512, LDS_BYTES, stream>>>((const char*)d_in[0], (const char*)d_in[1], (const char*)d_in[2],
                                             (const char*)d_in[3], (const char*)d_in[4], (char*)d_out, (char*)d_ws);
}
```

```cpp
#define PROBE2 3
#define PBLK 100
#define PM 0
#define PRE 256
#define POST 0
#define POSTM 0
#include <hip/hip_runtime.h>
#include <stdint.h>

typedef unsigned u32x4 __attribute__((ext_vector_type(4)));
typedef unsigned u32x2 __attribute__((ext_vector_type(2)));
typedef float f32x4 __attribute__((ext_vector_type(4)));
typedef float f32x2 __attribute__((ext_vector_type(2)));
typedef __bf16 bf16x2 __attribute__((ext_vector_type(2)));
typedef __bf16 bf16x8 __attribute__((ext_vector_type(8)));
typedef short s16x4 __attribute__((ext_vector_type(4)));
typedef short s16x8 __attribute__((ext_vector_type(8)));
#define LAS __attribute__((address_space(3)))

#define WIN 5
#define AHEAD 48
#define CATCH 4
#define QWIN 2
#define WO_AT 2
#define TRIG (31 - WIN)
#define KAUX 2
#define VAUX 2
#define QSLEEP 4
#define G1SLEEP 2
#define G2SLEEP 8
#define FBACK 1
#define FSLEEP 2

constexpr int NB = 16, DM = 1024, NH = 32, SK = 2048, DH = 128, HD = NH * DH;
constexpr unsigned KV_BYTES = 1u << 28;
constexpr unsigned WS_Q = 4096, Q_STRIDE = 2048;
constexpr unsigned WS_PART = 1u << 20, PART_STRIDE = 4096 + 256;
constexpr unsigned WS_P = 4u << 20, P_STRIDE = 8192;
constexpr unsigned WS_SPAN = 8u << 20;
constexpr unsigned OOB = 0x80000000u;
constexpr unsigned WS_PROF = 16u << 20;
constexpr int L_WQ = 0;
constexpr int L_QP = L_WQ + 4 * 8192;
constexpr int L_Q = L_QP + 4 * 2048;
constexpr int L_ACC = L_Q + 8 * 256;
constexpr int L_HDR = L_ACC + 16 * 4 * 512;
constexpr int O_ROW = 272;
constexpr int L_O = L_HDR + 16 * 4 * 16;
constexpr int L_CNT = L_O + 16 * O_ROW;
constexpr int C_QP = L_CNT, C_Q = L_CNT + 4, C_TRIG = L_CNT + 8, C_O = L_CNT + 12, C_EP = L_CNT + 16;
constexpr int L_END = L_CNT + 64;
constexpr int LDS_BYTES = 96 * 1024;
static_assert(L_END + 1024 <= LDS_BYTES, "lds");
constexpr unsigned SPIN_CAP = 1u << 15;

__device__ __forceinline__ float asf(unsigned u) { return __builtin_bit_cast(float, u); }
__device__ __forceinline__ unsigned asu(float f) { return __builtin_bit_cast(unsigned, f); }
__device__ __forceinline__ float dot2(unsigned a, unsigned b, float acc) {
  return __builtin_amdgcn_fdot2_f32_bf16(__builtin_bit_cast(bf16x2, a), __builtin_bit_cast(bf16x2, b), acc, false);
}
template <int CTRL> __device__ __forceinline__ float dpp(float x) {
  return asf((unsigned)__builtin_amdgcn_mov_dpp((int)asu(x), CTRL, 0xf, 0xf, true));
}
constexpr int QX1 = 0xB1, QX2 = 0x4E, HMIR = 0x141, ROR4 = 0x124, ROR8 = 0x128, BCAST = 0x150;
__device__ __forceinline__ unsigned pack_bf16(float lo, float hi) {
  bf16x2 r; r[0] = (__bf16)lo; r[1] = (__bf16)hi; return __builtin_bit_cast(unsigned, r);
}
__device__ __forceinline__ float fold16(float a, float b) {
  auto r = __builtin_amdgcn_permlane16_swap(asu(a), asu(b), false, false); return asf(r[0]) + asf(r[1]);
}
__device__ __forceinline__ float fold32(float a, float b) {
  auto r = __builtin_amdgcn_permlane32_swap(asu(a), asu(b), false, false); return asf(r[0]) + asf(r[1]);
}
__device__ __forceinline__ float max16(float a) {
  auto r = __builtin_amdgcn_permlane16_swap(asu(a), asu(a), false, false); return fmaxf(asf(r[0]), asf(r[1]));
}
__device__ __forceinline__ float max32(float a) {
  auto r = __builtin_amdgcn_permlane32_swap(asu(a), asu(a), false, false); return fmaxf(asf(r[0]), asf(r[1]));
}
__device__ __forceinline__ __amdgpu_buffer_rsrc_t mkrsrc(const void* p, unsigned bytes) {
  return __builtin_amdgcn_make_buffer_rsrc((void*)p, 0, (int)bytes, 0x00020000);
}
template <typename T> __device__ __forceinline__ LAS T* lds_at(LAS char* s, int off) { return (LAS T*)(s + off); }
#define CBAR() asm volatile("" ::: "memory")
#define SCHED() __builtin_amdgcn_sched_barrier(0)

#define STAMP(slot) do { unsigned t_ = (unsigned)__builtin_amdgcn_s_memrealtime(); if (lane == 0) *lds_at<volatile unsigned>(smem, L_END + (wave * 32 + (slot)) * 4) = t_; } while (0)

__device__ __forceinline__ void lds_wait_ge(LAS char* smem, int off, unsigned want) {
#pragma unroll 1
  for (unsigned spins = 0; spins < SPIN_CAP; ++spins) {
    unsigned v = *lds_at<volatile unsigned>(smem, off);
    if ((unsigned)__builtin_amdgcn_readfirstlane((int)v) >= want) break;
    __builtin_amdgcn_s_sleep(1);
  }
  CBAR();
}

template <int SLEEP>
__device__ __forceinline__ void lds_wait_flags(LAS char* smem, int u, int lane) {
#pragma unroll 1
  for (unsigned spins = 0; spins < SPIN_CAP; ++spins) {
    const unsigned f = *lds_at<volatile unsigned>(smem, L_HDR + (u * 4 + (lane & 3)) * 16 + 8);
    if (__builtin_amdgcn_ballot_w64(f != 1u) == 0) break;
    __builtin_amdgcn_s_sleep(SLEEP);
  }
  CBAR();
}

__device__ __forceinline__ bf16x8 colfrag(const u32x4* r, int t) {
  u32x4 f;
#pragma unroll
  for (int u = 0; u < 4; ++u)
    f[u] = __builtin_amdgcn_perm(r[2 * u + 1][t >> 1], r[2 * u][t >> 1], (t & 1) ? 0x07060302u : 0x05040100u);
  return __builtin_bit_cast(bf16x8, f);
}

struct Tag { unsigned t[4]; };
__device__ __forceinline__ u32x4 tag_pack(f32x4 v, const Tag& g) {
  u32x4 r;
#pragma unroll
  for (int i = 0; i < 4; ++i) r[i] = (asu(v[i]) & 0xfffffc00u) | g.t[i];
  return r;
}
__device__ __forceinline__ unsigned tag_bad(u32x4 v, const Tag& g) {
  return ((v[0] ^ g.t[0]) | (v[1] ^ g.t[1]) | (v[2] ^ g.t[2]) | (v[3] ^ g.t[3]));
}
__device__ __forceinline__ f32x4 untag(u32x4 v) {
  f32x4 r;
#pragma unroll
  for (int i = 0; i < 4; ++i) r[i] = asf(v[i] & 0xfffffc00u);
  return r;
}

__device__ __forceinline__ unsigned make_tag(unsigned ep, Tag& tg) {
  const unsigned e1 = (unsigned)__builtin_amdgcn_readfirstlane((int)ep) + 1u;
  tg.t[0] = e1 & 1023u; tg.t[1] = (e1 + (e1 >> 10)) & 1023u; tg.t[2] = (e1 + (e1 >> 20)) & 1023u; tg.t[3] = (e1 + (e1 >> 30) + 341u) & 1023u;
  asm volatile("" :: "s"(e1) : "memory");
  return e1;
}
__device__ __forceinline__ float bcast16(float x, int l) {
  switch (l) {
    case 0: return dpp<BCAST + 0>(x); case 1: return dpp<BCAST + 1>(x); case 2: return dpp<BCAST + 2>(x); case 3: return dpp<BCAST + 3>(x);
    case 4: return dpp<BCAST + 4>(x); case 5: return dpp<BCAST + 5>(x); case 6: return dpp<BCAST + 6>(x); case 7: return dpp<BCAST + 7>(x);
    case 8: return dpp<BCAST + 8>(x); case 9: return dpp<BCAST + 9>(x); case 10: return dpp<BCAST + 10>(x); case 11: return dpp<BCAST + 11>(x);
    case 12: return dpp<BCAST + 12>(x); case 13: return dpp<BCAST + 13>(x); case 14: return dpp<BCAST + 14>(x); default: return dpp<BCAST + 15>(x);
  }
}

struct Stream { __amdgpu_buffer_rsrc_t rk, rv; unsigned voff; };
__device__ __forceinline__ constexpr unsigned unit_off(int u) { return ((unsigned)(u & 1) << 17) | ((unsigned)(u >> 1) << 25); }
__device__ __forceinline__ u32x4 kv_load(const Stream& s, int n, unsigned sb) {
  const int i = n & 15;
  const unsigned so = sb + (unsigned)(((i >> 2) & 1) << 15) + (unsigned)(((i >> 3) & 1) << 14);
  const unsigned vo = s.voff + (unsigned)((i & 3) << 10);
  return (n & 16) ? __builtin_amdgcn_raw_buffer_load_b128(s.rv, vo, so, VAUX) : __builtin_amdgcn_raw_buffer_load_b128(s.rk, vo, so, KAUX);
}

constexpr int issue_upto(int n) {
  return (n + WIN + 1 > AHEAD + WIN + n / CATCH) ? n + WIN + 1 : AHEAD + WIN + n / CATCH;
}
static_assert(issue_upto(63) == 64 + WIN, "catch-up must end within two batches");

__device__ __forceinline__ void lds_inc(LAS char* smem, int off, int lane) {
  CBAR();
  if (lane == 0) __hip_atomic_fetch_add(lds_at<unsigned>(smem, off), 1u, __ATOMIC_RELAXED, __HIP_MEMORY_SCOPE_WORKGROUP);
  CBAR();
}

template <int MODE, int N>
__device__ __forceinline__ void att_batch(const Stream& s, u32x4 (&buf)[N], unsigned sb, unsigned sbn, int b, LAS char* smem, int wave, int lane) {
  constexpr float C2 = 0.088388347648318440f * 1.4426950408889634f;
  const int c = lane & 15, r = lane >> 4;
  const u32x4 qv = *lds_at<u32x4>(smem, L_Q + (b >> 1) * 256 + c * 16);
  const bool b3 = (lane & 8) != 0, b2 = (lane & 4) != 0;
  float a[2], u[2], v[4], p[4];
  f32x2 acc[4];
  float mneg = 0.f;
#pragma unroll
  for (int n = 0; n < 32; ++n) {
    constexpr int OFF = MODE == 1 ? 32 : 0;
    if (MODE >= 2) {
      asm volatile("" :: "v"(buf[n]));
      if (MODE == 2 || n + WIN < 32) buf[(n + WIN) & 31] = kv_load(s, (n + WIN) & 31, n + WIN < 32 ? sb : sbn);
      if (MODE == 3 && n == TRIG) lds_inc(smem, C_TRIG, lane);
    } else {
      const int g = n + OFF;
#pragma unroll
      for (int i = (g == 0 ? AHEAD + WIN : issue_upto(g - 1)); i < issue_upto(g); ++i) {
        asm volatile("" :: "v"(buf[i - WIN]));
        buf[i] = kv_load(s, i & 31, sb + unit_off(i >> 5));
      }
    }
    SCHED();
    const u32x4 d = buf[n + OFF];
    if (n < 16) {
      float t = dot2(d[0], qv[0], 0.f);
      t = dot2(d[1], qv[1], t); t = dot2(d[2], qv[2], t); t = dot2(d[3], qv[3], t);
      a[n & 1] = t;
      if ((n & 1) == 1) {
        const float keep = b3 ? a[1] : a[0], send = b3 ? a[0] : a[1];
        u[(n >> 1) & 1] = keep + dpp<ROR8>(send);
      }
      if ((n & 3) == 3) {
        const float keep = b2 ? u[1] : u[0], send = b2 ? u[0] : u[1];
        float x = keep + dpp<HMIR>(send);
        x += dpp<QX1>(x); x += dpp<QX2>(x);
        v[n >> 2] = x;
      }
      if (n == 15) {
        float m = fmaxf(fmaxf(v[0], v[1]), fmaxf(v[2], v[3]));
        m = fmaxf(m, dpp<ROR4>(m)); m = fmaxf(m, dpp<ROR8>(m));
        m = max16(m); m = max32(m);
        mneg = -m * C2;
#pragma unroll
        for (int k = 0; k < 4; ++k) p[k] = __builtin_amdgcn_exp2f(fmaf(v[k], C2, mneg));
      }
    } else {
      const int i = n - 16;
      float pb;
      switch (i & 3) {
        case 0: pb = dpp<BCAST + 0>(p[i >> 2]); break;
        case 1: pb = dpp<BCAST + 8>(p[i >> 2]); break;
        case 2: pb = dpp<BCAST + 4>(p[i >> 2]); break;
        default: pb = dpp<BCAST + 12>(p[i >> 2]); break;
      }
      const f32x2 pp = {pb, pb};
#pragma unroll
      for (int j = 0; j < 4; ++j) {
        const f32x2 x = {asf(d[j] << 16), asf(d[j] & 0xffff0000u)};
        acc[j] = (i == 0) ? pp * x : __builtin_elementwise_fma(pp, x, acc[j]);
      }
    }
    SCHED();
  }
  float l = (p[0] + p[1]) + (p[2] + p[3]);
  l += dpp<ROR4>(l); l += dpp<ROR8>(l);
  l = fold16(l, l); l = fold32(l, l);
  f32x2 o;
  o[0] = fold32(fold16(acc[0][0], acc[1][0]), fold16(acc[2][0], acc[3][0]));
  o[1] = fold32(fold16(acc[0][1], acc[1][1]), fold16(acc[2][1], acc[3][1]));
  *lds_at<volatile f32x2>(smem, L_ACC + (b * 4 + wave) * 512 + c * 32 + r * 8) = o;
  CBAR();
  if (lane == 0) {
    u32x4 h = {asu(-mneg), asu(l), 1u, 0u};
    *lds_at<volatile u32x4>(smem, L_HDR + (b * 4 + wave) * 16) = h;
  }
  CBAR();
}

template <int SLEEP>
__device__ __forceinline__ void merge8(LAS char* smem, int pp, int lane, f32x4& o, float& lse) {
  u32x4 hd; f32x4 pa[8];
  if (SLEEP) lds_wait_flags<SLEEP>(smem, 2 * pp + 1, lane);
#pragma unroll 1
  for (unsigned spins = 0; spins < SPIN_CAP; ++spins) {
    hd = *lds_at<volatile u32x4>(smem, L_HDR + (pp * 8 + (lane & 7)) * 16);
#pragma unroll
    for (int w = 0; w < 8; ++w) pa[w] = *lds_at<volatile f32x4>(smem, L_ACC + (pp * 8 + w) * 512 + (lane & 31) * 16);
    if (__builtin_amdgcn_ballot_w64(hd[2] != 1u) == 0) break;
  }
  const float m = asf(hd[0]);
  float M = fmaxf(m, dpp<QX1>(m)); M = fmaxf(M, dpp<QX2>(M)); M = fmaxf(M, dpp<HMIR>(M));
  const float e = __builtin_amdgcn_exp2f(m - M);
  float L = e * asf(hd[1]); L += dpp<QX1>(L); L += dpp<QX2>(L); L += dpp<HMIR>(L);
  const float en = e * __builtin_amdgcn_rcpf(L);
  o = pa[0] * bcast16(en, 0);
#pragma unroll
  for (int w = 1; w < 8; ++w) o += pa[w] * bcast16(en, w);
  lse = M + __builtin_amdgcn_logf(L);
}
__device__ __forceinline__ f32x4 lse_chunk(float lse) {
  const float hi = asf(asu(lse) & 0xfffffc00u);
  return f32x4{hi, lse - hi, 0.f, 0.f};
}

template <int N, int SLEEP, int BACK>
__device__ __forceinline__ void gather(u32x4 (&v)[N], __amdgpu_buffer_rsrc_t rws, unsigned (&off)[N], const unsigned (&so)[N], const Tag& tg) {
#pragma unroll
  for (int n = 0; n < N; ++n) v[n] = u32x4{0u, 0u, 0u, 0u};
#pragma unroll 1
  for (unsigned spins = 0; spins < SPIN_CAP; ++spins) {
    CBAR();
    u32x4 t[N];
#pragma unroll
    for (int n = 0; n < N; ++n) t[n] = __builtin_amdgcn_raw_buffer_load_b128(rws, off[n], so[n], 16);
    int left = 0;
#pragma unroll
    for (int n = 0; n < N; ++n) {
      const bool want = off[n] != OOB;
      if (want) v[n] = t[n];
      const bool miss = want && (tag_bad(t[n], tg) & 1023u) != 0;
      off[n] = miss ? off[n] : OOB;
      left += __builtin_popcountll(__builtin_amdgcn_ballot_w64(miss));
    }
    if (left == 0) break;
    if (SLEEP) __builtin_amdgcn_s_sleep(SLEEP);
    if (BACK) for (int i = left >> 4; i > 0; --i) __builtin_amdgcn_s_sleep(BACK);
  }
}

__global__ void __launch_bounds__(512, 2)
mha_fused(const char* __restrict__ x, const char* __restrict__ kc, const char* __restrict__ vc,
          const char* __restrict__ wq, const char* __restrict__ wo, char* __restrict__ out, char* __restrict__ ws) {
  extern __shared__ char smem_raw[];
  LAS char* smem = (LAS char*)smem_raw;
  const int lane = threadIdx.x & 63;
  const int wave = __builtin_amdgcn_readfirstlane(threadIdx.x >> 6);
  const int blk = blockIdx.x;
  const int h = (blk & 7) * 4 + (blk >> 6), j = (blk >> 3) & 7;
  const int par = (j >> 1) & 1;
  STAMP(0);

  if (wave < 4) {
    Stream s;
    s.rk = mkrsrc(kc, KV_BYTES); s.rv = mkrsrc(vc, KV_BYTES); s.voff = lane * 16;
    const unsigned base = ((unsigned)h << 19) | ((unsigned)(j & 5) << 16) | ((unsigned)par << 24) | ((unsigned)wave << 12);
    u32x4 ring[32];
    {
      u32x4 buf[64 + WIN];
#pragma unroll
      for (int n = 0; n < WIN; ++n) buf[n] = kv_load(s, n, base);
      SCHED();
      __builtin_amdgcn_s_barrier();
      SCHED();
#pragma unroll
      for (int n = 0; n < AHEAD; ++n) {
        asm volatile("" :: "v"(buf[n]));
        buf[n + WIN] = kv_load(s, (n + WIN) & 31, base + unit_off((n + WIN) >> 5));
        SCHED();
      }
      STAMP(1);
      lds_wait_ge(smem, C_Q, 1);
      STAMP(2);
      att_batch<0>(s, buf, base, 0, 0, smem, wave, lane);
      STAMP(3);
      att_batch<1>(s, buf, base, 0, 1, smem, wave, lane);
      STAMP(4);
#pragma unroll
      for (int n = 0; n < WIN; ++n) ring[n] = buf[64 + n];
    }
    lds_wait_ge(smem, C_Q, 2);
    for (int u = 2; u < 15; ++u) {
      att_batch<2>(s, ring, base + unit_off(u), base + unit_off(u + 1), u, smem, wave, lane);
      STAMP(3 + u);
    }
    att_batch<3>(s, ring, base + unit_off(15), 0, 15, smem, wave, lane);
    STAMP(18);
  } else {
    const int sw = wave - 4;
    const int c = lane & 15, g = lane >> 4, half = lane >> 5;
    const __amdgpu_buffer_rsrc_t rws = mkrsrc(ws, WS_SPAN);
    Tag tg;
    unsigned e1;
    unsigned prevEnd = 0;
    if (sw == 0) {
      const __amdgpu_buffer_rsrc_t rp = mkrsrc(ws + (32u << 20), 1u << 20);
      const unsigned my0 = (unsigned)__builtin_amdgcn_s_memrealtime();
      if (lane == 0) __builtin_amdgcn_raw_buffer_store_b32(my0, rp, blk * 128, 0, 16);
      if (blk == PBLK) { prevEnd = my0 - 50000u; for (int i = 0; i < 4; ++i) { const unsigned t = __builtin_amdgcn_raw_buffer_load_b32(rp, (i * 64 + lane) * 128 + 65536, 0, 16); if (my0 - t < my0 - prevEnd) prevEnd = t; } }
    }
    {
      const __amdgpu_buffer_rsrc_t rq = mkrsrc(wq, DM * HD * 2), rx = mkrsrc(x, NB * DM * 2);
      const int row0 = 512 * (j >> 2) + 128 * sw;
      u32x4 wb[8], xf[4];
#pragma unroll
      for (int i = 0; i < 8; ++i)
        wb[i] = __builtin_amdgcn_raw_buffer_load_b128(rq, (lane >> 2) * (HD * 2) + (lane & 3) * 16, (row0 + 16 * i) * (HD * 2) + h * 256 + (j & 3) * 64, 2);
#pragma unroll
      for (int k = 0; k < 4; ++k) xf[k] = __builtin_amdgcn_raw_buffer_load_b128(rx, c * (DM * 2) + g * 16 + k * 64, row0 * 2, 0);
      const unsigned ep = __builtin_amdgcn_raw_buffer_load_b32(rws, 0, 0, 16);
      SCHED();
      if (sw == 0) {
        if (lane < 8) *lds_at<volatile unsigned>(smem, L_CNT + lane * 4) = 0u;
        *lds_at<volatile u32x4>(smem, L_HDR + lane * 16) = u32x4{0u, 0u, 0u, 0u};
        asm volatile("s_waitcnt lgkmcnt(0)" ::: "memory");
      }
      __builtin_amdgcn_s_barrier();
      SCHED();
#pragma unroll
      for (int i = 0; i < 8; ++i) *lds_at<volatile u32x4>(smem, L_WQ + sw * 8192 + i * 1024 + lane * 16) = wb[i];
      CBAR();
      f32x4 acc[2] = {f32x4{0.f, 0.f, 0.f, 0.f}, f32x4{0.f, 0.f, 0.f, 0.f}};
#pragma unroll
      for (int k = 0; k < 4; ++k)
#pragma unroll
        for (int t = 0; t < 2; ++t) {
          const int ad = L_WQ + sw * 8192 + (32 * k + 8 * g + (c >> 2)) * 64 + t * 32 + (c & 3) * 8;
          const s16x4 lo = __builtin_amdgcn_ds_read_tr16_b64_v4i16(lds_at<s16x4>(smem, ad));
          const s16x4 hi = __builtin_amdgcn_ds_read_tr16_b64_v4i16(lds_at<s16x4>(smem, ad + 256));
          const s16x8 fr = __builtin_shufflevector(lo, hi, 0, 1, 2, 3, 4, 5, 6, 7);
          acc[t] = __builtin_amdgcn_mfma_f32_16x16x32_bf16(__builtin_bit_cast(bf16x8, fr), __builtin_bit_cast(bf16x8, xf[k]), acc[t], 0, 0, 0);
        }
      *lds_at<volatile f32x4>(smem, L_QP + sw * 2048 + lane * 16) = acc[0];
      *lds_at<volatile f32x4>(smem, L_QP + sw * 2048 + 1024 + lane * 16) = acc[1];
      e1 = make_tag(ep, tg);
      lds_inc(smem, C_QP, lane);
      STAMP(1);
      if (sw < 2) {
        lds_wait_ge(smem, C_QP, 4);
        f32x4 sum = {0.f, 0.f, 0.f, 0.f};
#pragma unroll
        for (int w = 0; w < 4; ++w) sum += *lds_at<volatile f32x4>(smem, L_QP + w * 2048 + ((lane & 7) * 16 + 8 * sw + (lane >> 3)) * 16);
        __builtin_amdgcn_raw_buffer_store_b128(tag_pack(sum, tg), rws, lane * 16, WS_Q + (h * 8 + j) * Q_STRIDE + sw * 1024, 16);
      }
    }
    const unsigned grp = WS_PART + h * 8 * PART_STRIDE;
    if (sw == 0) {
      {
        const unsigned qo = (lane >> 3) * Q_STRIDE + (lane & 7) * 16, qs = WS_Q + h * 8 * Q_STRIDE + par * 128;
        {
          u32x4 v[1]; unsigned off[1] = {qo}; const unsigned so[1] = {qs};
          gather<1, QSLEEP, 0>(v, rws, off, so, tg);
          const f32x4 f = untag(v[0]);
          *lds_at<volatile unsigned>(smem, L_Q + (lane & 31) * 8 + half * 4) = pack_bf16(fold32(f[0], f[2]), fold32(f[1], f[3]));
          lds_inc(smem, C_Q, lane);
        }
        STAMP(2);
        u32x4 v[7]; unsigned off[7], so[7];
#pragma unroll
        for (int n = 0; n < 7; ++n) { off[n] = qo; so[n] = qs + (n + 1) * 256; }
        gather<7, QSLEEP, 0>(v, rws, off, so, tg);
#pragma unroll
        for (int n = 0; n < 7; ++n) {
          const f32x4 f = untag(v[n]);
          *lds_at<volatile unsigned>(smem, L_Q + (n + 1) * 256 + (lane & 31) * 8 + half * 4) = pack_bf16(fold32(f[0], f[2]), fold32(f[1], f[3]));
        }
        lds_inc(smem, C_Q, lane);
        STAMP(3);
      }
      u32x4 msg[4]; f32x4 lch = {0.f, 0.f, 0.f, 0.f};
#pragma unroll
      for (int i = 0; i < 4; ++i) {
        f32x4 o; float lse;
        merge8<16>(smem, i < 3 ? 2 * i + half : 6, lane, o, lse);
        msg[i] = tag_pack(o, tg);
        if ((lane & 31) == i) lch = lse_chunk(lse);
        asm volatile("" : "+v"(msg[i]), "+v"(lch));
      }
      const unsigned mine = grp + (par * 4 + (j & 1) + 2 * (j >> 2)) * PART_STRIDE;
      lds_wait_flags<16>(smem, 14, lane);
      lds_wait_ge(smem, C_TRIG, 4);
#pragma unroll
      for (int i = 0; i < 4; ++i)
        __builtin_amdgcn_raw_buffer_store_b128(msg[i], rws, (i < 3 || lane < 32) ? lane * 16u : OOB, mine + i * 1024, 16);
      __builtin_amdgcn_raw_buffer_store_b128(tag_pack(lch, tg), rws, (lane & 31) < 4 ? ((lane & 31) * 2 + half) * 16u : OOB, mine + 4096, 16);
      STAMP(17);
      {
        f32x4 o; float lse;
        merge8<0>(smem, 7, lane, o, lse);
        if (lane >= 32) o = lse_chunk(lse);
        __builtin_amdgcn_raw_buffer_store_b128(tag_pack(o, tg), rws, lane < 32 ? lane * 16u : lane < 40 ? lane * 16u + 128u : OOB, mine + 7 * 512, 16);
      }
      STAMP(18);
      lds_wait_ge(smem, C_O, 3);
      u32x4 v[8]; unsigned off[8], so[8];
#pragma unroll
      for (int n = 0; n < 8; ++n) { off[n] = g * P_STRIDE + c * 16; so[n] = WS_P + (j * NH + 4 * n) * P_STRIDE + h * 256; }
      gather<8, FSLEEP, FBACK>(v, rws, off, so, tg);
      f32x4 sum = untag(v[0]);
#pragma unroll
      for (int n = 1; n < 8; ++n) sum += untag(v[n]);
      const float y = fold32(fold16(sum[0], sum[1]), fold16(sum[2], sum[3]));
      auto sw16 = __builtin_amdgcn_permlane16_swap(asu(y), asu(y), false, false);
      const unsigned pk = pack_bf16(asf(sw16[0]), asf(sw16[1]));
      if ((g & 1) == 0) *(unsigned*)(out + c * (DM * 2) + (j * 128 + 32 * (h & 3) + 4 * (h >> 2) + g) * 2) = pk;
      if (blk == 0 && lane == 0) __builtin_amdgcn_raw_buffer_store_b32(e1, rws, 0, 0, 16);
      STAMP(20);
      {
        const __amdgpu_buffer_rsrc_t rp = mkrsrc(ws + (32u << 20), 1u << 20);
        if (blk == PBLK) {
          unsigned mn = 0x7fffffffu, mx = 0u, pe = 0u;
          const unsigned my0 = (unsigned)__builtin_amdgcn_readfirstlane((int)*lds_at<volatile unsigned>(smem, L_END));
          for (int i = 0; i < 4; ++i) {
            const unsigned t = __builtin_amdgcn_raw_buffer_load_b32(rp, (i * 64 + lane) * 128, 0, 16) - my0 + 100000u;
            mn = t < mn ? t : mn; mx = t > mx ? t : mx;
          }
          pe = prevEnd - my0 + 100000u;
          for (int o = 32; o > 0; o >>= 1) { unsigned a = __shfl_xor(mn, o), b = __shfl_xor(mx, o), c2 = __shfl_xor(pe, o); mn = a < mn ? a : mn; mx = b > mx ? b : mx; pe = c2 > pe ? c2 : pe; }
          mn = __builtin_amdgcn_readfirstlane(mn); mx = __builtin_amdgcn_readfirstlane(mx); pe = __builtin_amdgcn_readfirstlane(pe);
          const unsigned t0_ = (unsigned)__builtin_amdgcn_s_memrealtime();
          unsigned X = PROBE2 == 1 ? mx - mn : PROBE2 == 2 ? t0_ - my0 + 100000u - mn : PROBE2 == 3 ? mn - pe : 100000u - mn;
          if (X > 10000u) X = 0u;
#pragma unroll 1
          for (unsigned i = 0; i < (1u << 20); ++i) { if ((unsigned)__builtin_amdgcn_s_memrealtime() - t0_ >= X * PM) break; __builtin_amdgcn_s_sleep(1); }
        }
        if (lane == 0) __builtin_amdgcn_raw_buffer_store_b32((unsigned)__builtin_amdgcn_s_memrealtime(), rp, blk * 128 + 65536, 0, 16);
      }
    } else if (sw == 1) {
      const __amdgpu_buffer_rsrc_t ro = mkrsrc(wo, HD * DM * 2);
      bf16x8 af[4][8];
      lds_wait_flags<16>(smem, WO_AT, lane);
#pragma unroll
      for (int k = 0; k < 4; ++k) {
        u32x4 wb[8];
#pragma unroll
        for (int i = 0; i < 8; ++i)
          wb[i] = __builtin_amdgcn_raw_buffer_load_b128(ro, g * (8 * DM * 2) + c * 16, (h * DH + k * 32 + i) * (DM * 2) + j * 256, 2);
#pragma unroll
        for (int t = 0; t < 8; ++t) { af[k][t] = colfrag(wb, t); asm volatile("" : "+v"(af[k][t])); }
        SCHED();
      }
      lds_wait_flags<16>(smem, 14, lane);
      lds_wait_flags<1>(smem, 15, lane);
      u32x4 v[5]; unsigned off[5], so[5];
#pragma unroll
      for (int i = 0; i < 4; ++i) { off[i] = half * 4 * PART_STRIDE + (lane & 31) * 16; so[i] = grp + i * PART_STRIDE + 7 * 512; }
      off[4] = (half * 4 + (lane & 3)) * PART_STRIDE; so[4] = grp + 4096 + 128;
      gather<5, G1SLEEP, 0>(v, rws, off, so, tg);
      const f32x4 lf = untag(v[4]);
      float M = fmaxf(lf[0], dpp<QX1>(lf[0])); M = fmaxf(M, dpp<QX2>(M));
      const float e = __builtin_amdgcn_exp2f((lf[0] - M) + lf[1]);
      float S = e + dpp<QX1>(e); S += dpp<QX2>(S);
      const float en = e * __builtin_amdgcn_rcpf(S);
      const f32x4 o = untag(v[0]) * dpp<0x00>(en) + untag(v[1]) * dpp<0x55>(en) + untag(v[2]) * dpp<0xAA>(en) + untag(v[3]) * dpp<0xFF>(en);
      *lds_at<volatile u32x2>(smem, L_O + (14 + half) * O_ROW + (lane & 31) * 8) = u32x2{pack_bf16(o[0], o[1]), pack_bf16(o[2], o[3])};
      STAMP(18);
      lds_wait_ge(smem, C_O, 2);
      STAMP(19);
      u32x4 of[4];
#pragma unroll
      for (int k = 0; k < 4; ++k) of[k] = *lds_at<volatile u32x4>(smem, L_O + c * O_ROW + k * 64 + g * 16);
      SCHED();
      f32x4 acc[8];
#pragma unroll
      for (int k = 0; k < 4; ++k)
#pragma unroll
        for (int t = 0; t < 8; ++t)
          acc[t] = __builtin_amdgcn_mfma_f32_16x16x32_bf16(af[k][t], __builtin_bit_cast(bf16x8, of[k]), k == 0 ? f32x4{0.f, 0.f, 0.f, 0.f} : acc[t], 0, 0, 0);
#pragma unroll
      for (int n = 0; n < 8; ++n) {
        const int i = n >> 1, th = n & 1;
        const f32x4 pv = {acc[4 * th][i], acc[4 * th + 1][i], acc[4 * th + 2][i], acc[4 * th + 3][i]};
        __builtin_amdgcn_raw_buffer_store_b128(tag_pack(pv, tg), rws, lane * 16, WS_P + (j * NH + h) * P_STRIDE + n * 1024, 16);
      }
      lds_inc(smem, C_O, lane);
      STAMP(20);
    } else {
      const int p0 = (sw - 2) * 4;
      lds_wait_flags<16>(smem, 14, lane);
      lds_wait_ge(smem, C_TRIG, 4);
      u32x4 lv[1];
      {
        unsigned off[1] = {(half * 4 + (lane & 3)) * PART_STRIDE + ((lane >> 2) & 3) * 16}; const unsigned so[1] = {grp + 4096 + p0 * 16};
        gather<1, G2SLEEP, 0>(lv, rws, off, so, tg);
      }
      STAMP(17);
      u32x4 v[16]; unsigned off[16], so[16];
#pragma unroll
      for (int n = 0; n < 16; ++n) {
        off[n] = (n >= 12 && sw == 3) ? OOB : half * 4 * PART_STRIDE + (lane & 31) * 16;
        so[n] = grp + (n & 3) * PART_STRIDE + (p0 + (n >> 2)) * 512;
      }
      gather<16, 2, 0>(v, rws, off, so, tg);
      const f32x4 lf = untag(lv[0]);
      float M = fmaxf(lf[0], dpp<QX1>(lf[0])); M = fmaxf(M, dpp<QX2>(M));
      const float e = __builtin_amdgcn_exp2f((lf[0] - M) + lf[1]);
      float S = e + dpp<QX1>(e); S += dpp<QX2>(S);
      const float en = e * __builtin_amdgcn_rcpf(S);
#pragma unroll
      for (int p = 0; p < 4; ++p) {
        f32x4 o = untag(v[4 * p]) * bcast16(en, 4 * p);
#pragma unroll
        for (int m = 1; m < 4; ++m) o += untag(v[4 * p + m]) * bcast16(en, 4 * p + m);
        if (!(p == 3 && sw == 3))
          *lds_at<volatile u32x2>(smem, L_O + (2 * (p0 + p) + half) * O_ROW + (lane & 31) * 8) = u32x2{pack_bf16(o[0], o[1]), pack_bf16(o[2], o[3])};
      }
      lds_inc(smem, C_O, lane);
      STAMP(18);
    }
  }
}

__global__ void pre_k(char* ws) {
  const __amdgpu_buffer_rsrc_t rp = mkrsrc(ws + (32u << 20), 1u << 20);
  const unsigned t = (unsigned)__builtin_amdgcn_s_memrealtime();
  if (gridDim.x == 1) { for (int i = 0; i < 4; ++i) __builtin_amdgcn_raw_buffer_store_b32(t, rp, (i * 64 + threadIdx.x) * 128 + 65536, 0, 16); }
  else if (threadIdx.x == 0) __builtin_amdgcn_raw_buffer_store_b32(t, rp, blockIdx.x * 128 + 65536, 0, 16);
}
__global__ void post_k(char* ws) {
  const __amdgpu_buffer_rsrc_t rp = mkrsrc(ws + (32u << 20), 1u << 20);
  const unsigned my0 = (unsigned)__builtin_amdgcn_s_memrealtime();
  unsigned d = 50000u;
  for (int i = 0; i < 4; ++i) { const unsigned t = __builtin_amdgcn_raw_buffer_load_b32(rp, (i * 64 + threadIdx.x) * 128 + 65536, 0, 16); if (my0 - t < d) d = my0 - t; }
  for (int o = 32; o > 0; o >>= 1) { unsigned a = __shfl_xor(d, o); d = a < d ? a : d; }
  d = __builtin_amdgcn_readfirstlane(d);
  if (d > 10000u) d = 0;
  const unsigned t0_ = (unsigned)__builtin_amdgcn_s_memrealtime();
#pragma unroll 1
  for (unsigned i = 0; i < (1u << 20); ++i) { if ((unsigned)__builtin_amdgcn_s_memrealtime() - t0_ >= d * POSTM) break; __builtin_amdgcn_s_sleep(1); }
}
extern "C" void kernel_launch(void* const* d_in, const int* in_sizes, int n_in, void* d_out, int out_size, void* d_ws, size_t ws_size, hipStream_t stream) {
  (void)hipFuncSetAttribute((const void*)mha_fused, hipFuncAttributeMaxDynamicSharedMemorySize, LDS_BYTES);
  if (PRE) pre_k<<<PRE, 64, 0, stream>>>((char*)d_ws);
  mha_fused<<<256, 512, LDS_BYTES, stream>>>((const char*)d_in[0], (const char*)d_in[1], (const char*)d_in[2],
                                             (const char*)d_in[3], (const char*)d_in[4], (char*)d_out, (char*)d_ws);
  if (POST) post_k<<<1, 64, 0, stream>>>((char*)d_ws);
}
```

```cpp
#define PROBE 1
#define PBLK 100
#define PW1 0
#define PS1 0
#define PW0 0
#define PS0 0
#define PM 0
#include <hip/hip_runtime.h>
#include <stdint.h>

typedef unsigned u32x4 __attribute__((ext_vector_type(4)));
typedef unsigned u32x2 __attribute__((ext_vector_type(2)));
typedef float f32x4 __attribute__((ext_vector_type(4)));
typedef float f32x2 __attribute__((ext_vector_type(2)));
typedef __bf16 bf16x2 __attribute__((ext_vector_type(2)));
typedef __bf16 bf16x8 __attribute__((ext_vector_type(8)));
typedef short s16x4 __attribute__((ext_vector_type(4)));
typedef short s16x8 __attribute__((ext_vector_type(8)));
#define LAS __attribute__((address_space(3)))

#define WIN 5
#define AHEAD 48
#define CATCH 4
#define QWIN 2
#define WO_AT 2
#define TRIG (31 - WIN)
#define KAUX 2
#define VAUX 2
#define QSLEEP 4
#define G1SLEEP 2
#define G2SLEEP 8
#define FBACK 1
#define FSLEEP 2

constexpr int NB = 16, DM = 1024, NH = 32, SK = 2048, DH = 128, HD = NH * DH;
constexpr unsigned KV_BYTES = 1u << 28;
constexpr unsigned WS_Q = 4096, Q_STRIDE = 2048;
constexpr unsigned WS_PART = 1u << 20, PART_STRIDE = 4096 + 256;
constexpr unsigned WS_P = 4u << 20, P_STRIDE = 8192;
constexpr unsigned WS_SPAN = 8u << 20;
constexpr unsigned OOB = 0x80000000u;
constexpr unsigned WS_PROF = 16u << 20;
constexpr int L_WQ = 0;
constexpr int L_QP = L_WQ + 4 * 8192;
constexpr int L_Q = L_QP + 4 * 2048;
constexpr int L_ACC = L_Q + 8 * 256;
constexpr int L_HDR = L_ACC + 16 * 4 * 512;
constexpr int O_ROW = 272;
constexpr int L_O = L_HDR + 16 * 4 * 16;
constexpr int L_CNT = L_O + 16 * O_ROW;
constexpr int C_QP = L_CNT, C_Q = L_CNT + 4, C_TRIG = L_CNT + 8, C_O = L_CNT + 12, C_EP = L_CNT + 16;
constexpr int L_END = L_CNT + 64;
constexpr int LDS_BYTES = 96 * 1024;
static_assert(L_END + 1024 <= LDS_BYTES, "lds");
constexpr unsigned SPIN_CAP = 1u << 15;

__device__ __forceinline__ float asf(unsigned u) { return __builtin_bit_cast(float, u); }
__device__ __forceinline__ unsigned asu(float f) { return __builtin_bit_cast(unsigned, f); }
__device__ __forceinline__ float dot2(unsigned a, unsigned b, float acc) {
  return __builtin_amdgcn_fdot2_f32_bf16(__builtin_bit_cast(bf16x2, a), __builtin_bit_cast(bf16x2, b), acc, false);
}
template <int CTRL> __device__ __forceinline__ float dpp(float x) {
  return asf((unsigned)__builtin_amdgcn_mov_dpp((int)asu(x), CTRL, 0xf, 0xf, true));
}
constexpr int QX1 = 0xB1, QX2 = 0x4E, HMIR = 0x141, ROR4 = 0x124, ROR8 = 0x128, BCAST = 0x150;
__device__ __forceinline__ unsigned pack_bf16(float lo, float hi) {
  bf16x2 r; r[0] = (__bf16)lo; r[1] = (__bf16)hi; return __builtin_bit_cast(unsigned, r);
}
__device__ __forceinline__ float fold16(float a, float b) {
  auto r = __builtin_amdgcn_permlane16_swap(asu(a), asu(b), false, false); return asf(r[0]) + asf(r[1]);
}
__device__ __forceinline__ float fold32(float a, float b) {
  auto r = __builtin_amdgcn_permlane32_swap(asu(a), asu(b), false, false); return asf(r[0]) + asf(r[1]);
}
__device__ __forceinline__ float max16(float a) {
  auto r = __builtin_amdgcn_permlane16_swap(asu(a), asu(a), false, false); return fmaxf(asf(r[0]), asf(r[1]));
}
__device__ __forceinline__ float max32(float a) {
  auto r = __builtin_amdgcn_permlane32_swap(asu(a), asu(a), false, false); return fmaxf(asf(r[0]), asf(r[1]));
}
__device__ __forceinline__ __amdgpu_buffer_rsrc_t mkrsrc(const void* p, unsigned bytes) {
  return __builtin_amdgcn_make_buffer_rsrc((void*)p, 0, (int)bytes, 0x00020000);
}
template <typename T> __device__ __forceinline__ LAS T* lds_at(LAS char* s, int off) { return (LAS T*)(s + off); }
#define CBAR() asm volatile("" ::: "memory")
#define SCHED() __builtin_amdgcn_sched_barrier(0)

#define STAMP(slot) do { unsigned t_ = (unsigned)__builtin_amdgcn_s_memrealtime(); if (lane == 0) *lds_at<volatile unsigned>(smem, L_END + (wave * 32 + (slot)) * 4) = t_; } while (0)

__device__ __forceinline__ void lds_wait_ge(LAS char* smem, int off, unsigned want) {
#pragma unroll 1
  for (unsigned spins = 0; spins < SPIN_CAP; ++spins) {
    unsigned v = *lds_at<volatile unsigned>(smem, off);
    if ((unsigned)__builtin_amdgcn_readfirstlane((int)v) >= want) break;
    __builtin_amdgcn_s_sleep(1);
  }
  CBAR();
}

template <int SLEEP>
__device__ __forceinline__ void lds_wait_flags(LAS char* smem, int u, int lane) {
#pragma unroll 1
  for (unsigned spins = 0; spins < SPIN_CAP; ++spins) {
    const unsigned f = *lds_at<volatile unsigned>(smem, L_HDR + (u * 4 + (lane & 3)) * 16 + 8);
    if (__builtin_amdgcn_ballot_w64(f != 1u) == 0) break;
    __builtin_amdgcn_s_sleep(SLEEP);
  }
  CBAR();
}

__device__ __forceinline__ bf16x8 colfrag(const u32x4* r, int t) {
  u32x4 f;
#pragma unroll
  for (int u = 0; u < 4; ++u)
    f[u] = __builtin_amdgcn_perm(r[2 * u + 1][t >> 1], r[2 * u][t >> 1], (t & 1) ? 0x07060302u : 0x05040100u);
  return __builtin_bit_cast(bf16x8, f);
}

struct Tag { unsigned t[4]; };
__device__ __forceinline__ u32x4 tag_pack(f32x4 v, const Tag& g) {
  u32x4 r;
#pragma unroll
  for (int i = 0; i < 4; ++i) r[i] = (asu(v[i]) & 0xfffffc00u) | g.t[i];
  return r;
}
__device__ __forceinline__ unsigned tag_bad(u32x4 v, const Tag& g) {
  return ((v[0] ^ g.t[0]) | (v[1] ^ g.t[1]) | (v[2] ^ g.t[2]) | (v[3] ^ g.t[3]));
}
__device__ __forceinline__ f32x4 untag(u32x4 v) {
  f32x4 r;
#pragma unroll
  for (int i = 0; i < 4; ++i) r[i] = asf(v[i] & 0xfffffc00u);
  return r;
}

__device__ __forceinline__ unsigned make_tag(unsigned ep, Tag& tg) {
  const unsigned e1 = (unsigned)__builtin_amdgcn_readfirstlane((int)ep) + 1u;
  tg.t[0] = e1 & 1023u; tg.t[1] = (e1 + (e1 >> 10)) & 1023u; tg.t[2] = (e1 + (e1 >> 20)) & 1023u; tg.t[3] = (e1 + (e1 >> 30) + 341u) & 1023u;
  asm volatile("" :: "s"(e1) : "memory");
  return e1;
}
__device__ __forceinline__ float bcast16(float x, int l) {
  switch (l) {
    case 0: return dpp<BCAST + 0>(x); case 1: return dpp<BCAST + 1>(x); case 2: return dpp<BCAST + 2>(x); case 3: return dpp<BCAST + 3>(x);
    case 4: return dpp<BCAST + 4>(x); case 5: return dpp<BCAST + 5>(x); case 6: return dpp<BCAST + 6>(x); case 7: return dpp<BCAST + 7>(x);
    case 8: return dpp<BCAST + 8>(x); case 9: return dpp<BCAST + 9>(x); case 10: return dpp<BCAST + 10>(x); case 11: return dpp<BCAST + 11>(x);
    case 12: return dpp<BCAST + 12>(x); case 13: return dpp<BCAST + 13>(x); case 14: return dpp<BCAST + 14>(x); default: return dpp<BCAST + 15>(x);
  }
}

struct Stream { __amdgpu_buffer_rsrc_t rk, rv; unsigned voff; };
__device__ __forceinline__ constexpr unsigned unit_off(int u) { return ((unsigned)(u & 1) << 17) | ((unsigned)(u >> 1) << 25); }
__device__ __forceinline__ u32x4 kv_load(const Stream& s, int n, unsigned sb) {
  const int i = n & 15;
  const unsigned so = sb + (unsigned)(((i >> 2) & 1) << 15) + (unsigned)(((i >> 3) & 1) << 14);
  const unsigned vo = s.voff + (unsigned)((i & 3) << 10);
  return (n & 16) ? __builtin_amdgcn_raw_buffer_load_b128(s.rv, vo, so, VAUX) : __builtin_amdgcn_raw_buffer_load_b128(s.rk, vo, so, KAUX);
}

constexpr int issue_upto(int n) {
  return (n + WIN + 1 > AHEAD + WIN + n / CATCH) ? n + WIN + 1 : AHEAD + WIN + n / CATCH;
}
static_assert(issue_upto(63) == 64 + WIN, "catch-up must end within two batches");

__device__ __forceinline__ void lds_inc(LAS char* smem, int off, int lane) {
  CBAR();
  if (lane == 0) __hip_atomic_fetch_add(lds_at<unsigned>(smem, off), 1u, __ATOMIC_RELAXED, __HIP_MEMORY_SCOPE_WORKGROUP);
  CBAR();
}

template <int MODE, int N>
__device__ __forceinline__ void att_batch(const Stream& s, u32x4 (&buf)[N], unsigned sb, unsigned sbn, int b, LAS char* smem, int wave, int lane) {
  constexpr float C2 = 0.088388347648318440f * 1.4426950408889634f;
  const int c = lane & 15, r = lane >> 4;
  const u32x4 qv = *lds_at<u32x4>(smem, L_Q + (b >> 1) * 256 + c * 16);
  const bool b3 = (lane & 8) != 0, b2 = (lane & 4) != 0;
  float a[2], u[2], v[4], p[4];
  f32x2 acc[4];
  float mneg = 0.f;
#pragma unroll
  for (int n = 0; n < 32; ++n) {
    constexpr int OFF = MODE == 1 ? 32 : 0;
    if (MODE >= 2) {
      asm volatile("" :: "v"(buf[n]));
      if (MODE == 2 || n + WIN < 32) buf[(n + WIN) & 31] = kv_load(s, (n + WIN) & 31, n + WIN < 32 ? sb : sbn);
      if (MODE == 3 && n == TRIG) lds_inc(smem, C_TRIG, lane);
    } else {
      const int g = n + OFF;
#pragma unroll
      for (int i = (g == 0 ? AHEAD + WIN : issue_upto(g - 1)); i < issue_upto(g); ++i) {
        asm volatile("" :: "v"(buf[i - WIN]));
        buf[i] = kv_load(s, i & 31, sb + unit_off(i >> 5));
      }
    }
    SCHED();
    const u32x4 d = buf[n + OFF];
    if (n < 16) {
      float t = dot2(d[0], qv[0], 0.f);
      t = dot2(d[1], qv[1], t); t = dot2(d[2], qv[2], t); t = dot2(d[3], qv[3], t);
      a[n & 1] = t;
      if ((n & 1) == 1) {
        const float keep = b3 ? a[1] : a[0], send = b3 ? a[0] : a[1];
        u[(n >> 1) & 1] = keep + dpp<ROR8>(send);
      }
      if ((n & 3) == 3) {
        const float keep = b2 ? u[1] : u[0], send = b2 ? u[0] : u[1];
        float x = keep + dpp<HMIR>(send);
        x += dpp<QX1>(x); x += dpp<QX2>(x);
        v[n >> 2] = x;
      }
      if (n == 15) {
        float m = fmaxf(fmaxf(v[0], v[1]), fmaxf(v[2], v[3]));
        m = fmaxf(m, dpp<ROR4>(m)); m = fmaxf(m, dpp<ROR8>(m));
        m = max16(m); m = max32(m);
        mneg = -m * C2;
#pragma unroll
        for (int k = 0; k < 4; ++k) p[k] = __builtin_amdgcn_exp2f(fmaf(v[k], C2, mneg));
      }
    } else {
      const int i = n - 16;
      float pb;
      switch (i & 3) {
        case 0: pb = dpp<BCAST + 0>(p[i >> 2]); break;
        case 1: pb = dpp<BCAST + 8>(p[i >> 2]); break;
        case 2: pb = dpp<BCAST + 4>(p[i >> 2]); break;
        default: pb = dpp<BCAST + 12>(p[i >> 2]); break;
      }
      const f32x2 pp = {pb, pb};
#pragma unroll
      for (int j = 0; j < 4; ++j) {
        const f32x2 x = {asf(d[j] << 16), asf(d[j] & 0xffff0000u)};
        acc[j] = (i == 0) ? pp * x : __builtin_elementwise_fma(pp, x, acc[j]);
      }
    }
    SCHED();
  }
  float l = (p[0] + p[1]) + (p[2] + p[3]);
  l += dpp<ROR4>(l); l += dpp<ROR8>(l);
  l = fold16(l, l); l = fold32(l, l);
  f32x2 o;
  o[0] = fold32(fold16(acc[0][0], acc[1][0]), fold16(acc[2][0], acc[3][0]));
  o[1] = fold32(fold16(acc[0][1], acc[1][1]), fold16(acc[2][1], acc[3][1]));
  *lds_at<volatile f32x2>(smem, L_ACC + (b * 4 + wave) * 512 + c * 32 + r * 8) = o;
  CBAR();
  if (lane == 0) {
    u32x4 h = {asu(-mneg), asu(l), 1u, 0u};
    *lds_at<volatile u32x4>(smem, L_HDR + (b * 4 + wave) * 16) = h;
  }
  CBAR();
}

template <int SLEEP>
__device__ __forceinline__ void merge8(LAS char* smem, int pp, int lane, f32x4& o, float& lse) {
  u32x4 hd; f32x4 pa[8];
  if (SLEEP) lds_wait_flags<SLEEP>(smem, 2 * pp + 1, lane);
#pragma unroll 1
  for (unsigned spins = 0; spins < SPIN_CAP; ++spins) {
    hd = *lds_at<volatile u32x4>(smem, L_HDR + (pp * 8 + (lane & 7)) * 16);
#pragma unroll
    for (int w = 0; w < 8; ++w) pa[w] = *lds_at<volatile f32x4>(smem, L_ACC + (pp * 8 + w) * 512 + (lane & 31) * 16);
    if (__builtin_amdgcn_ballot_w64(hd[2] != 1u) == 0) break;
  }
  const float m = asf(hd[0]);
  float M = fmaxf(m, dpp<QX1>(m)); M = fmaxf(M, dpp<QX2>(M)); M = fmaxf(M, dpp<HMIR>(M));
  const float e = __builtin_amdgcn_exp2f(m - M);
  float L = e * asf(hd[1]); L += dpp<QX1>(L); L += dpp<QX2>(L); L += dpp<HMIR>(L);
  const float en = e * __builtin_amdgcn_rcpf(L);
  o = pa[0] * bcast16(en, 0);
#pragma unroll
  for (int w = 1; w < 8; ++w) o += pa[w] * bcast16(en, w);
  lse = M + __builtin_amdgcn_logf(L);
}
__device__ __forceinline__ f32x4 lse_chunk(float lse) {
  const float hi = asf(asu(lse) & 0xfffffc00u);
  return f32x4{hi, lse - hi, 0.f, 0.f};
}

template <int N, int SLEEP, int BACK>
__device__ __forceinline__ void gather(u32x4 (&v)[N], __amdgpu_buffer_rsrc_t rws, unsigned (&off)[N], const unsigned (&so)[N], const Tag& tg) {
#pragma unroll
  for (int n = 0; n < N; ++n) v[n] = u32x4{0u, 0u, 0u, 0u};
#pragma unroll 1
  for (unsigned spins = 0; spins < SPIN_CAP; ++spins) {
    CBAR();
    u32x4 t[N];
#pragma unroll
    for (int n = 0; n < N; ++n) t[n] = __builtin_amdgcn_raw_buffer_load_b128(rws, off[n], so[n], 16);
    int left = 0;
#pragma unroll
    for (int n = 0; n < N; ++n) {
      const bool want = off[n] != OOB;
      if (want) v[n] = t[n];
      const bool miss = want && (tag_bad(t[n], tg) & 1023u) != 0;
      off[n] = miss ? off[n] : OOB;
      left += __builtin_popcountll(__builtin_amdgcn_ballot_w64(miss));
    }
    if (left == 0) break;
    if (SLEEP) __builtin_amdgcn_s_sleep(SLEEP);
    if (BACK) for (int i = left >> 4; i > 0; --i) __builtin_amdgcn_s_sleep(BACK);
  }
}

__global__ void __launch_bounds__(512, 2)
mha_fused(const char* __restrict__ x, const char* __restrict__ kc, const char* __restrict__ vc,
          const char* __restrict__ wq, const char* __restrict__ wo, char* __restrict__ out, char* __restrict__ ws) {
  extern __shared__ char smem_raw[];
  LAS char* smem = (LAS char*)smem_raw;
  const int lane = threadIdx.x & 63;
  const int wave = __builtin_amdgcn_readfirstlane(threadIdx.x >> 6);
  const int blk = blockIdx.x;
  const int h = (blk & 7) * 4 + (blk >> 6), j = (blk >> 3) & 7;
  const int par = (j >> 1) & 1;
  STAMP(0);

  if (wave < 4) {
    Stream s;
    s.rk = mkrsrc(kc, KV_BYTES); s.rv = mkrsrc(vc, KV_BYTES); s.voff = lane * 16;
    const unsigned base = ((unsigned)h << 19) | ((unsigned)(j & 5) << 16) | ((unsigned)par << 24) | ((unsigned)wave << 12);
    u32x4 ring[32];
    {
      u32x4 buf[64 + WIN];
#pragma unroll
      for (int n = 0; n < WIN; ++n) buf[n] = kv_load(s, n, base);
      SCHED();
      STAMP(21);
      SCHED();
      __builtin_amdgcn_s_barrier();
      SCHED();
#pragma unroll
      for (int n = 0; n < AHEAD; ++n) {
        asm volatile("" :: "v"(buf[n]));
        buf[n + WIN] = kv_load(s, (n + WIN) & 31, base + unit_off((n + WIN) >> 5));
        SCHED();
        if (n == 0) { STAMP(22); SCHED(); }
        if (n == 15) { STAMP(23); SCHED(); }
      }
      STAMP(1);
      lds_wait_ge(smem, C_Q, 1);
      STAMP(2);
      att_batch<0>(s, buf, base, 0, 0, smem, wave, lane);
      STAMP(3);
      att_batch<1>(s, buf, base, 0, 1, smem, wave, lane);
      STAMP(4);
#pragma unroll
      for (int n = 0; n < WIN; ++n) ring[n] = buf[64 + n];
    }
    lds_wait_ge(smem, C_Q, 2);
    for (int u = 2; u < 15; ++u) {
      att_batch<2>(s, ring, base + unit_off(u), base + unit_off(u + 1), u, smem, wave, lane);
      STAMP(3 + u);
    }
    att_batch<3>(s, ring, base + unit_off(15), 0, 15, smem, wave, lane);
    STAMP(18);
  } else {
    const int sw = wave - 4;
    const int c = lane & 15, g = lane >> 4, half = lane >> 5;
    const __amdgpu_buffer_rsrc_t rws = mkrsrc(ws, WS_SPAN);
    Tag tg;
    unsigned e1;
    {
      const __amdgpu_buffer_rsrc_t rq = mkrsrc(wq, DM * HD * 2), rx = mkrsrc(x, NB * DM * 2);
      const int row0 = 512 * (j >> 2) + 128 * sw;
      u32x4 wb[8], xf[4];
#pragma unroll
      for (int i = 0; i < 8; ++i)
        wb[i] = __builtin_amdgcn_raw_buffer_load_b128(rq, (lane >> 2) * (HD * 2) + (lane & 3) * 16, (row0 + 16 * i) * (HD * 2) + h * 256 + (j & 3) * 64, 2);
#pragma unroll
      for (int k = 0; k < 4; ++k) xf[k] = __builtin_amdgcn_raw_buffer_load_b128(rx, c * (DM * 2) + g * 16 + k * 64, row0 * 2, 0);
      const unsigned ep = __builtin_amdgcn_raw_buffer_load_b32(rws, 0, 0, 16);
      SCHED();
      if (sw == 0) {
        if (lane < 8) *lds_at<volatile unsigned>(smem, L_CNT + lane * 4) = 0u;
        *lds_at<volatile u32x4>(smem, L_HDR + lane * 16) = u32x4{0u, 0u, 0u, 0u};
        asm volatile("s_waitcnt lgkmcnt(0)" ::: "memory");
      }
      __builtin_amdgcn_s_barrier();
      SCHED();
#pragma unroll
      for (int i = 0; i < 8; ++i) *lds_at<volatile u32x4>(smem, L_WQ + sw * 8192 + i * 1024 + lane * 16) = wb[i];
      CBAR();
      f32x4 acc[2] = {f32x4{0.f, 0.f, 0.f, 0.f}, f32x4{0.f, 0.f, 0.f, 0.f}};
#pragma unroll
      for (int k = 0; k < 4; ++k)
#pragma unroll
        for (int t = 0; t < 2; ++t) {
          const int ad = L_WQ + sw * 8192 + (32 * k + 8 * g + (c >> 2)) * 64 + t * 32 + (c & 3) * 8;
          const s16x4 lo = __builtin_amdgcn_ds_read_tr16_b64_v4i16(lds_at<s16x4>(smem, ad));
          const s16x4 hi = __builtin_amdgcn_ds_read_tr16_b64_v4i16(lds_at<s16x4>(smem, ad + 256));
          const s16x8 fr = __builtin_shufflevector(lo, hi, 0, 1, 2, 3, 4, 5, 6, 7);
          acc[t] = __builtin_amdgcn_mfma_f32_16x16x32_bf16(__builtin_bit_cast(bf16x8, fr), __builtin_bit_cast(bf16x8, xf[k]), acc[t], 0, 0, 0);
        }
      *lds_at<volatile f32x4>(smem, L_QP + sw * 2048 + lane * 16) = acc[0];
      *lds_at<volatile f32x4>(smem, L_QP + sw * 2048 + 1024 + lane * 16) = acc[1];
      e1 = make_tag(ep, tg);
      lds_inc(smem, C_QP, lane);
      STAMP(1);
      if (sw < 2) {
        lds_wait_ge(smem, C_QP, 4);
        f32x4 sum = {0.f, 0.f, 0.f, 0.f};
#pragma unroll
        for (int w = 0; w < 4; ++w) sum += *lds_at<volatile f32x4>(smem, L_QP + w * 2048 + ((lane & 7) * 16 + 8 * sw + (lane >> 3)) * 16);
        __builtin_amdgcn_raw_buffer_store_b128(tag_pack(sum, tg), rws, lane * 16, WS_Q + (h * 8 + j) * Q_STRIDE + sw * 1024, 16);
      }
    }
    const unsigned grp = WS_PART + h * 8 * PART_STRIDE;
    if (sw == 0) {
      {
        const unsigned qo = (lane >> 3) * Q_STRIDE + (lane & 7) * 16, qs = WS_Q + h * 8 * Q_STRIDE + par * 128;
        {
          u32x4 v[1]; unsigned off[1] = {qo}; const unsigned so[1] = {qs};
          gather<1, QSLEEP, 0>(v, rws, off, so, tg);
          const f32x4 f = untag(v[0]);
          *lds_at<volatile unsigned>(smem, L_Q + (lane & 31) * 8 + half * 4) = pack_bf16(fold32(f[0], f[2]), fold32(f[1], f[3]));
          lds_inc(smem, C_Q, lane);
        }
        STAMP(2);
        u32x4 v[7]; unsigned off[7], so[7];
#pragma unroll
        for (int n = 0; n < 7; ++n) { off[n] = qo; so[n] = qs + (n + 1) * 256; }
        gather<7, QSLEEP, 0>(v, rws, off, so, tg);
#pragma unroll
        for (int n = 0; n < 7; ++n) {
          const f32x4 f = untag(v[n]);
          *lds_at<volatile unsigned>(smem, L_Q + (n + 1) * 256 + (lane & 31) * 8 + half * 4) = pack_bf16(fold32(f[0], f[2]), fold32(f[1], f[3]));
        }
        lds_inc(smem, C_Q, lane);
        STAMP(3);
      }
      u32x4 msg[4]; f32x4 lch = {0.f, 0.f, 0.f, 0.f};
#pragma unroll
      for (int i = 0; i < 4; ++i) {
        f32x4 o; float lse;
        merge8<16>(smem, i < 3 ? 2 * i + half : 6, lane, o, lse);
        msg[i] = tag_pack(o, tg);
        if ((lane & 31) == i) lch = lse_chunk(lse);
        asm volatile("" : "+v"(msg[i]), "+v"(lch));
      }
      const unsigned mine = grp + (par * 4 + (j & 1) + 2 * (j >> 2)) * PART_STRIDE;
      lds_wait_flags<16>(smem, 14, lane);
      lds_wait_ge(smem, C_TRIG, 4);
#pragma unroll
      for (int i = 0; i < 4; ++i)
        __builtin_amdgcn_raw_buffer_store_b128(msg[i], rws, (i < 3 || lane < 32) ? lane * 16u : OOB, mine + i * 1024, 16);
      __builtin_amdgcn_raw_buffer_store_b128(tag_pack(lch, tg), rws, (lane & 31) < 4 ? ((lane & 31) * 2 + half) * 16u : OOB, mine + 4096, 16);
      STAMP(17);
      {
        f32x4 o; float lse;
        merge8<0>(smem, 7, lane, o, lse);
        if (lane >= 32) o = lse_chunk(lse);
        __builtin_amdgcn_raw_buffer_store_b128(tag_pack(o, tg), rws, lane < 32 ? lane * 16u : lane < 40 ? lane * 16u + 128u : OOB, mine + 7 * 512, 16);
      }
      STAMP(18);
      lds_wait_ge(smem, C_O, 3);
      u32x4 v[8]; unsigned off[8], so[8];
#pragma unroll
      for (int n = 0; n < 8; ++n) { off[n] = g * P_STRIDE + c * 16; so[n] = WS_P + (j * NH + 4 * n) * P_STRIDE + h * 256; }
      gather<8, FSLEEP, FBACK>(v, rws, off, so, tg);
      f32x4 sum = untag(v[0]);
#pragma unroll
      for (int n = 1; n < 8; ++n) sum += untag(v[n]);
      const float y = fold32(fold16(sum[0], sum[1]), fold16(sum[2], sum[3]));
      auto sw16 = __builtin_amdgcn_permlane16_swap(asu(y), asu(y), false, false);
      const unsigned pk = pack_bf16(asf(sw16[0]), asf(sw16[1]));
      if ((g & 1) == 0) *(unsigned*)(out + c * (DM * 2) + (j * 128 + 32 * (h & 3) + 4 * (h >> 2) + g) * 2) = pk;
      if (blk == 0 && lane == 0) __builtin_amdgcn_raw_buffer_store_b32(e1, rws, 0, 0, 16);
      STAMP(20);
      if (blk == PBLK) {
        auto st = [&](int w, int sl) { return (unsigned)__builtin_amdgcn_readfirstlane((int)*lds_at<volatile unsigned>(smem, L_END + (w * 32 + sl) * 4)); };
        unsigned X = st(PW1, PS1) - st(PW0, PS0);
        if (X > 30000u) X = 30000u;
        const unsigned t0_ = (unsigned)__builtin_amdgcn_s_memrealtime();
#pragma unroll 1
        for (unsigned i = 0; i < (1u << 20); ++i) { if ((unsigned)__builtin_amdgcn_s_memrealtime() - t0_ >= X * PM) break; __builtin_amdgcn_s_sleep(1); }
      }
    } else if (sw == 1) {
      const __amdgpu_buffer_rsrc_t ro = mkrsrc(wo, HD * DM * 2);
      bf16x8 af[4][8];
      lds_wait_flags<16>(smem, WO_AT, lane);
#pragma unroll
      for (int k = 0; k < 4; ++k) {
        u32x4 wb[8];
#pragma unroll
        for (int i = 0; i < 8; ++i)
          wb[i] = __builtin_amdgcn_raw_buffer_load_b128(ro, g * (8 * DM * 2) + c * 16, (h * DH + k * 32 + i) * (DM * 2) + j * 256, 2);
#pragma unroll
        for (int t = 0; t < 8; ++t) { af[k][t] = colfrag(wb, t); asm volatile("" : "+v"(af[k][t])); }
        SCHED();
      }
      lds_wait_flags<16>(smem, 14, lane);
      lds_wait_flags<1>(smem, 15, lane);
      u32x4 v[5]; unsigned off[5], so[5];
#pragma unroll
      for (int i = 0; i < 4; ++i) { off[i] = half * 4 * PART_STRIDE + (lane & 31) * 16; so[i] = grp + i * PART_STRIDE + 7 * 512; }
      off[4] = (half * 4 + (lane & 3)) * PART_STRIDE; so[4] = grp + 4096 + 128;
      gather<5, G1SLEEP, 0>(v, rws, off, so, tg);
      const f32x4 lf = untag(v[4]);
      float M = fmaxf(lf[0], dpp<QX1>(lf[0])); M = fmaxf(M, dpp<QX2>(M));
      const float e = __builtin_amdgcn_exp2f((lf[0] - M) + lf[1]);
      float S = e + dpp<QX1>(e); S += dpp<QX2>(S);
      const float en = e * __builtin_amdgcn_rcpf(S);
      const f32x4 o = untag(v[0]) * dpp<0x00>(en) + untag(v[1]) * dpp<0x55>(en) + untag(v[2]) * dpp<0xAA>(en) + untag(v[3]) * dpp<0xFF>(en);
      *lds_at<volatile u32x2>(smem, L_O + (14 + half) * O_ROW + (lane & 31) * 8) = u32x2{pack_bf16(o[0], o[1]), pack_bf16(o[2], o[3])};
      STAMP(18);
      lds_wait_ge(smem, C_O, 2);
      STAMP(19);
      u32x4 of[4];
#pragma unroll
      for (int k = 0; k < 4; ++k) of[k] = *lds_at<volatile u32x4>(smem, L_O + c * O_ROW + k * 64 + g * 16);
      SCHED();
      f32x4 acc[8];
#pragma unroll
      for (int k = 0; k < 4; ++k)
#pragma unroll
        for (int t = 0; t < 8; ++t)
          acc[t] = __builtin_amdgcn_mfma_f32_16x16x32_bf16(af[k][t], __builtin_bit_cast(bf16x8, of[k]), k == 0 ? f32x4{0.f, 0.f, 0.f, 0.f} : acc[t], 0, 0, 0);
#pragma unroll
      for (int n = 0; n < 8; ++n) {
        const int i = n >> 1, th = n & 1;
        const f32x4 pv = {acc[4 * th][i], acc[4 * th + 1][i], acc[4 * th + 2][i], acc[4 * th + 3][i]};
        __builtin_amdgcn_raw_buffer_store_b128(tag_pack(pv, tg), rws, lane * 16, WS_P + (j * NH + h) * P_STRIDE + n * 1024, 16);
      }
      lds_inc(smem, C_O, lane);
      STAMP(20);
    } else {
      const int p0 = (sw - 2) * 4;
      lds_wait_flags<16>(smem, 14, lane);
      lds_wait_ge(smem, C_TRIG, 4);
      u32x4 lv[1];
      {
        unsigned off[1] = {(half * 4 + (lane & 3)) * PART_STRIDE + ((lane >> 2) & 3) * 16}; const unsigned so[1] = {grp + 4096 + p0 * 16};
        gather<1, G2SLEEP, 0>(lv, rws, off, so, tg);
      }
      STAMP(17);
      u32x4 v[16]; unsigned off[16], so[16];
#pragma unroll
      for (int n = 0; n < 16; ++n) {
        off[n] = (n >= 12 && sw == 3) ? OOB : half * 4 * PART_STRIDE + (lane & 31) * 16;
        so[n] = grp + (n & 3) * PART_STRIDE + (p0 + (n >> 2)) * 512;
      }
      gather<16, 2, 0>(v, rws, off, so, tg);
      const f32x4 lf = untag(lv[0]);
      float M = fmaxf(lf[0], dpp<QX1>(lf[0])); M = fmaxf(M, dpp<QX2>(M));
      const float e = __builtin_amdgcn_exp2f((lf[0] - M) + lf[1]);
      float S = e + dpp<QX1>(e); S += dpp<QX2>(S);
      const float en = e * __builtin_amdgcn_rcpf(S);
#pragma unroll
      for (int p = 0; p < 4; ++p) {
        f32x4 o = untag(v[4 * p]) * bcast16(en, 4 * p);
#pragma unroll
        for (int m = 1; m < 4; ++m) o += untag(v[4 * p + m]) * bcast16(en, 4 * p + m);
        if (!(p == 3 && sw == 3))
          *lds_at<volatile u32x2>(smem, L_O + (2 * (p0 + p) + half) * O_ROW + (lane & 31) * 8) = u32x2{pack_bf16(o[0], o[1]), pack_bf16(o[2], o[3])};
      }
      lds_inc(smem, C_O, lane);
      STAMP(18);
    }
  }
}

extern "C" void kernel_launch(void* const* d_in, const int* in_sizes, int n_in, void* d_out, int out_size, void* d_ws, size_t ws_size, hipStream_t stream) {
  (void)hipFuncSetAttribute((const void*)mha_fused, hipFuncAttributeMaxDynamicSharedMemorySize, LDS_BYTES);
  mha_fused<<<256, 512, LDS_BYTES, stream>>>((const char*)d_in[0], (const char*)d_in[1], (const char*)d_in[2],
                                             (const char*)d_in[3], (const char*)d_in[4], (char*)d_out, (char*)d_ws);
}
```

```cpp
#include <hip/hip_runtime.h>
#include <stdint.h>

typedef unsigned u32x4 __attribute__((ext_vector_type(4)));
typedef unsigned u32x2 __attribute__((ext_vector_type(2)));
typedef float f32x4 __attribute__((ext_vector_type(4)));
typedef float f32x2 __attribute__((ext_vector_type(2)));
typedef __bf16 bf16x2 __attribute__((ext_vector_type(2)));
typedef __bf16 bf16x8 __attribute__((ext_vector_type(8)));
typedef short s16x4 __attribute__((ext_vector_type(4)));
typedef short s16x8 __attribute__((ext_vector_type(8)));
#define LAS __attribute__((address_space(3)))

constexpr int WIN = 12;
constexpr int AHEAD = 51 - WIN;
constexpr int CATCH = 4;
constexpr int WOGATE = 40, KAUX = 2, VAUX = 2;
constexpr int TAU_T = 429;
constexpr int TOFF = 310;
constexpr int SL0 = 31;
constexpr int SL = 8;
constexpr int TMIN = 416, TFLOOR = 400;
constexpr int MDLY = 180, U0 = 5;
constexpr float MISS = 480.f, MISSG = 90.f, PAIR = 0.3f, D0 = 50.f, DEAD = 22.f, CLEAN0 = 20.f, CLEAN = 12.f, B0 = 20.f;
constexpr float GAIN0 = 1.4f, GAIN = 1.2f, KEEP = 0.5f, KSL = 0.01f, KMAX = 0.6f, BF = 0.9f, SKG = 0.85f;
constexpr int QSLEEP = 4, LDLY = 40, LRETRY = 4, EDLY = 600, ERETRY = 64, GDLY = 30, FDLY = 60, FSLEEP = 2;

constexpr int NB = 16, DM = 1024, NH = 32, SK = 2048, DH = 128, HD = NH * DH;
constexpr unsigned KV_BYTES = 1u << 28;
constexpr unsigned WS_Q = 4096, Q_STRIDE = 2048;
constexpr unsigned WS_PART = 1u << 20, PART_SLOT = 512, PART_Q = 256 * PART_SLOT + 4096;
constexpr unsigned WS_P = 3u << 20, P_STRIDE = 8192, P_J = NH * P_STRIDE + 4096;
constexpr unsigned WS_L = 2u << 20, L_Q_ = 65536;
constexpr unsigned WS_SPAN = 8u << 20;
constexpr unsigned OOB = 0x80000000u;
constexpr int L_WQ = 0;
constexpr int L_QP = L_WQ + 4 * 8192;
constexpr int L_Q = L_QP + 4 * 2048;
constexpr int L_ACC = L_Q + 4 * 256;
constexpr int L_HDR = L_ACC + 4 * 4 * 512;
constexpr int O_ROW = 272;
constexpr int L_O = L_HDR + 4 * 4 * 16;
constexpr int L_CNT = L_O + 16 * O_ROW;
constexpr int C_QP = L_CNT, C_Q = L_CNT + 4, C_O = L_CNT + 12, C_EP = L_CNT + 16, C_WO = L_CNT + 20, C_EXT = L_CNT + 64;
constexpr int L_WO = 0, WO_ROW = 288;
static_assert(L_WO + 128 * WO_ROW <= L_Q, "wo tile");
constexpr int L_END = L_CNT + 256;
constexpr int LDS_BYTES = 96 * 1024;
static_assert(L_END <= LDS_BYTES, "lds");
constexpr unsigned SPIN_CAP = 1u << 15;

__device__ __forceinline__ float asf(unsigned u) { return __builtin_bit_cast(float, u); }
__device__ __forceinline__ unsigned asu(float f) { return __builtin_bit_cast(unsigned, f); }
__device__ __forceinline__ float dot2(unsigned a, unsigned b, float acc) {
  return __builtin_amdgcn_fdot2_f32_bf16(__builtin_bit_cast(bf16x2, a), __builtin_bit_cast(bf16x2, b), acc, false);
}
template <int CTRL> __device__ __forceinline__ float dpp(float x) {
  return asf((unsigned)__builtin_amdgcn_mov_dpp((int)asu(x), CTRL, 0xf, 0xf, true));
}
constexpr int QX1 = 0xB1, QX2 = 0x4E, HMIR = 0x141, ROR4 = 0x124, ROR8 = 0x128, BCAST = 0x150;
__device__ __forceinline__ unsigned pack_bf16(float lo, float hi) {
  bf16x2 r; r[0] = (__bf16)lo; r[1] = (__bf16)hi; return __builtin_bit_cast(unsigned, r);
}
__device__ __forceinline__ float fold16(float a, float b) {
  auto r = __builtin_amdgcn_permlane16_swap(asu(a), asu(b), false, false); return asf(r[0]) + asf(r[1]);
}
__device__ __forceinline__ float fold32(float a, float b) {
  auto r = __builtin_amdgcn_permlane32_swap(asu(a), asu(b), false, false); return asf(r[0]) + asf(r[1]);
}
__device__ __forceinline__ float max16(float a) {
  auto r = __builtin_amdgcn_permlane16_swap(asu(a), asu(a), false, false); return fmaxf(asf(r[0]), asf(r[1]));
}
__device__ __forceinline__ float max32(float a) {
  auto r = __builtin_amdgcn_permlane32_swap(asu(a), asu(a), false, false); return fmaxf(asf(r[0]), asf(r[1]));
}
__device__ __forceinline__ __amdgpu_buffer_rsrc_t mkrsrc(const void* p, unsigned bytes) {
  return __builtin_amdgcn_make_buffer_rsrc((void*)p, 0, (int)bytes, 0x00020000);
}
template <typename T> __device__ __forceinline__ LAS T* lds_at(LAS char* s, int off) { return (LAS T*)(s + off); }
#define CBAR() asm volatile("" ::: "memory")
#define SCHED() __builtin_amdgcn_sched_barrier(0)


template <int SLEEP = 1>
__device__ __forceinline__ void lds_wait_ge(LAS char* smem, int off, unsigned want) {
#pragma unroll 1
  for (unsigned spins = 0; spins < SPIN_CAP; ++spins) {
    unsigned v = *lds_at<volatile unsigned>(smem, off);
    if ((unsigned)__builtin_amdgcn_readfirstlane((int)v) >= want) break;
    if (SLEEP) __builtin_amdgcn_s_sleep(SLEEP);
  }
  CBAR();
}

template <int SLEEP>
__device__ __forceinline__ void lds_wait_flags(LAS char* smem, int q, int word, int lane) {
#pragma unroll 1
  for (unsigned spins = 0; spins < SPIN_CAP; ++spins) {
    const unsigned f = *lds_at<volatile unsigned>(smem, L_HDR + (q * 4 + (lane & 3)) * 16 + word * 4);
    if (__builtin_amdgcn_ballot_w64(f == 0u) == 0) break;
    __builtin_amdgcn_s_sleep(SLEEP);
  }
  CBAR();
}

struct Tag { unsigned t[4]; };
__device__ __forceinline__ u32x4 tag_pack(f32x4 v, const Tag& g) {
  u32x4 r;
#pragma unroll
  for (int i = 0; i < 4; ++i) r[i] = (asu(v[i]) & 0xfffffc00u) | g.t[i];
  return r;
}
__device__ __forceinline__ unsigned tag_bad(u32x4 v, const Tag& g) {
  return ((v[0] ^ g.t[0]) | (v[1] ^ g.t[1]) | (v[2] ^ g.t[2]) | (v[3] ^ g.t[3]));
}
__device__ __forceinline__ f32x4 untag(u32x4 v) {
  f32x4 r;
#pragma unroll
  for (int i = 0; i < 4; ++i) r[i] = asf(v[i] & 0xfffffc00u);
  return r;
}

__device__ __forceinline__ unsigned make_tag(unsigned ep, Tag& tg) {
  const unsigned e1 = (unsigned)__builtin_amdgcn_readfirstlane((int)ep) + 1u;
  tg.t[0] = e1 & 1023u; tg.t[1] = (e1 + (e1 >> 10)) & 1023u; tg.t[2] = (e1 + (e1 >> 20)) & 1023u; tg.t[3] = (e1 + (e1 >> 30) + 341u) & 1023u;
  asm volatile("" :: "s"(e1) : "memory");
  return e1;
}
struct Stream { __amdgpu_buffer_rsrc_t rk, rv, ro; unsigned voff, wvoff; };
struct Pace { unsigned from, now; int due, lim, step, fast, ext, mid; };
__device__ __forceinline__ void wait_until(unsigned due, unsigned now) {
  asm volatile("s_sub_u32 vcc_hi, %1, %0\n\ts_cmp_lt_i32 vcc_hi, 0\n\ts_cbranch_scc0 2f\n"
               "1:\n\ts_sleep 1\n\ts_memrealtime vcc\n\ts_waitcnt lgkmcnt(0)\n\ts_sub_u32 vcc_hi, vcc_lo, %0\n\ts_cmp_lt_i32 vcc_hi, 0\n\ts_cbranch_scc1 1b\n"
               "2:" :: "s"(due), "s"(now) : "vcc", "scc");
}
__device__ __forceinline__ void sleep_until(unsigned due) {
  asm volatile("1:\n\ts_sleep 4\n\ts_memrealtime vcc\n\ts_waitcnt lgkmcnt(0)\n\ts_sub_u32 vcc_hi, vcc_lo, %0\n\ts_cmp_lt_i32 vcc_hi, 0\n\ts_cbranch_scc1 1b"
               :: "s"(due) : "vcc", "scc");
}
__device__ __forceinline__ void pace_gate(Pace& p, int i, bool on = true) {
  if ((i & 1) == 0) {
    const int k = (i & 31) >> 1;
    p.from += (unsigned)((k + 1) * (TAU_T + SL0) / 16 - k * (TAU_T + SL0) / 16);
    if (on) wait_until(p.from, p.now);
  } else if (on) p.now = (unsigned)__builtin_amdgcn_s_memrealtime();
}
__device__ __forceinline__ void pace_var(Pace& p, int i, bool lim) {
  if ((i & 1) == 0) {
    p.due += p.step;
    const int tgt = lim ? max(p.due, p.lim) : p.due;
    wait_until(p.from + (unsigned)(tgt >> 4), p.now);
    const int at = (int)(p.now - p.from) << 4;
    if (lim) p.lim = max(tgt, at) + p.fast;
  } else p.now = (unsigned)__builtin_amdgcn_s_memrealtime();
}
__device__ __forceinline__ constexpr unsigned unit_off(int u) { return ((unsigned)(u & 3) << 17) | ((unsigned)(u & 4) << 23) | ((unsigned)(u & 8) << 24); }
__device__ __forceinline__ constexpr int batch_of(int q, int j) { return ((j >> 1) & 1) | ((q & 1) << 1) | ((j & 1) << 2) | ((q & 2) << 2); }
__device__ __forceinline__ u32x4 kv_load(const Stream& s, int n, unsigned sb) {
  const int i = n & 15;
  const unsigned so = sb + (unsigned)(((i >> 2) & 1) << 15) + (unsigned)(((i >> 3) & 1) << 14);
  const unsigned vo = s.voff + (unsigned)((i & 3) << 10);
  return (n & 16) ? __builtin_amdgcn_raw_buffer_load_b128(s.rv, vo, so, VAUX) : __builtin_amdgcn_raw_buffer_load_b128(s.rk, vo, so, KAUX);
}

constexpr int issue_upto(int n) {
  return (n + WIN + 1 > AHEAD + WIN + n / CATCH) ? n + WIN + 1 : AHEAD + WIN + n / CATCH;
}
static_assert(issue_upto(63) == 64 + WIN, "catch-up must end within two batches");

__device__ __forceinline__ void lds_inc(LAS char* smem, int off, int lane) {
  CBAR();
  if (lane == 0) __hip_atomic_fetch_add(lds_at<unsigned>(smem, off), 1u, __ATOMIC_RELAXED, __HIP_MEMORY_SCOPE_WORKGROUP);
  CBAR();
}

struct Run { float m, l; f32x2 acc[4]; };
template <int MODE, int N>
__device__ __forceinline__ void att_batch(const Stream& s, Pace& pc, Run& st, u32x4 (&buf)[N], unsigned sb, unsigned sbn, int b, LAS char* smem, int wave, int lane) {
  constexpr float C2 = 0.088388347648318440f * 1.4426950408889634f;
  const int c = lane & 15, r = lane >> 4;
  const u32x4 qv = *lds_at<u32x4>(smem, L_Q + (b >> 2) * 256 + c * 16);
  if (MODE >= 2) {
    const int e = *lds_at<volatile int>(smem, C_EXT + b * 4);
    const int up = __builtin_amdgcn_readfirstlane(e) - pc.ext;
    if ((b & 3) == 2) pc.mid = min(max((int)(pc.now - pc.from) - (pc.due >> 4), 0), 511) << 11;
    pc.fast = max(TMIN - (up >> 1), TFLOOR);
    pc.step = TAU_T + up; pc.ext += up;
  }
  const bool b3 = (lane & 8) != 0, b2 = (lane & 4) != 0;
  float a[2], u[2], v[4], p[4];
  f32x2 (&acc)[4] = st.acc;
  const bool flush = MODE == 3 || (MODE == 2 && (b & 3) == 3);
#pragma unroll
  for (int n = 0; n < 32; ++n) {
    constexpr int OFF = MODE == 1 ? 32 : 0;
    if (MODE >= 2) {
      asm volatile("" :: "v"(buf[n]));
      if (MODE == 2 || n + WIN < WOGATE) pace_var(pc, n, MODE == 2);
      if (MODE == 2 || n + WIN < 32) buf[(n + WIN) & 31] = kv_load(s, (n + WIN) & 31, n + WIN < 32 ? sb : sbn);
      if (MODE == 3 && n + WIN >= 32 && n + WIN < 40) {
        buf[(n + WIN) & 31] = __builtin_amdgcn_raw_buffer_load_b128(s.ro, s.wvoff, sbn + ((n + WIN) & 31) * 16 * DM * 2, 2);
      }
    } else {
      const int g = n + OFF;
#pragma unroll
      for (int i = (g == 0 ? AHEAD + WIN : issue_upto(g - 1)); i < issue_upto(g); ++i) {
        asm volatile("" :: "v"(buf[i - WIN]));
        pace_gate(pc, i - WIN, i - WIN + 1 >= g && issue_upto(g + 1) == g + WIN + 2);
        buf[i] = kv_load(s, i & 31, sb ^ unit_off(i >> 5));
      }
    }
    SCHED();
    const u32x4 d = buf[n + OFF];
    if (n < 16) {
      float t = dot2(d[0], qv[0], 0.f);
      t = dot2(d[1], qv[1], t); t = dot2(d[2], qv[2], t); t = dot2(d[3], qv[3], t);
      if ((n & 1) == 0) asm volatile("" : "+v"(t));
      a[n & 1] = t;
      if ((n & 1) == 1) {
        const float keep = b3 ? a[1] : a[0], send = b3 ? a[0] : a[1];
        u[(n >> 1) & 1] = keep + dpp<ROR8>(send);
        if ((n & 3) == 1) asm volatile("" : "+v"(u[0]));
      }
      if ((n & 3) == 3) {
        const float keep = b2 ? u[1] : u[0], send = b2 ? u[0] : u[1];
        float x = keep + dpp<HMIR>(send);
        x += dpp<QX1>(x); x += dpp<QX2>(x);
        asm volatile("" : "+v"(x));
        v[n >> 2] = x;
      }
      if (n == 15) {
        float m = fmaxf(fmaxf(v[0], v[1]), fmaxf(v[2], v[3]));
        m = fmaxf(m, dpp<ROR4>(m)); m = fmaxf(m, dpp<ROR8>(m));
        m = max16(m); m = max32(m);
        if (MODE != 0) m = fmaxf(m, st.m);
        const float mneg = -m * C2;
#pragma unroll
        for (int k = 0; k < 4; ++k) p[k] = __builtin_amdgcn_exp2f(fmaf(v[k], C2, mneg));
        float l = (p[0] + p[1]) + (p[2] + p[3]);
        if (MODE != 0) {
          const float sc = __builtin_amdgcn_exp2f(fmaf(st.m, C2, mneg));
          l = fmaf(st.l, sc, l);
#pragma unroll
          for (int j = 0; j < 4; ++j) acc[j] *= f32x2{sc, sc};
        }
        asm volatile("" : "+v"(l), "+v"(p[0]), "+v"(p[1]), "+v"(p[2]), "+v"(p[3]));
        st.m = m; st.l = l;
        if (flush) {
          l += dpp<ROR4>(l); l += dpp<ROR8>(l);
          l = fold16(l, l); l = fold32(l, l);
          if (lane == 0) *lds_at<volatile u32x4>(smem, L_HDR + ((b & ~3) + wave) * 16) = u32x4{asu(-mneg), asu(l), (unsigned)(min(max((int)(pc.now - pc.from) - (pc.due >> 4), 1), 511) | pc.mid), 0u};
        }
      }
    } else {
      const int i = n - 16;
      float pb;
      switch (i & 3) {
        case 0: pb = dpp<BCAST + 0>(p[i >> 2]); break;
        case 1: pb = dpp<BCAST + 8>(p[i >> 2]); break;
        case 2: pb = dpp<BCAST + 4>(p[i >> 2]); break;
        default: pb = dpp<BCAST + 12>(p[i >> 2]); break;
      }
      const f32x2 pp = {pb, pb};
#pragma unroll
      for (int j = 0; j < 4; ++j) {
        const f32x2 x = {asf(d[j] << 16), asf(d[j] & 0xffff0000u)};
        acc[j] = (MODE == 0 && i == 0) ? pp * x : __builtin_elementwise_fma(pp, x, acc[j]);
      }
      asm volatile("" : "+v"(acc[0]), "+v"(acc[1]), "+v"(acc[2]), "+v"(acc[3]));
    }
    SCHED();
  }
  if (flush) {
    f32x2 o;
    o[0] = fold32(fold16(acc[0][0], acc[1][0]), fold16(acc[2][0], acc[3][0]));
    o[1] = fold32(fold16(acc[0][1], acc[1][1]), fold16(acc[2][1], acc[3][1]));
    *lds_at<volatile f32x2>(smem, L_ACC + ((b & ~3) + wave) * 512 + c * 32 + r * 8) = o;
    CBAR();
    if (lane == 0) *lds_at<volatile unsigned>(smem, L_HDR + ((b & ~3) + wave) * 16 + 12) = 1u;
    CBAR();
    st.m = -__builtin_inff(); st.l = 0.f;
#pragma unroll
    for (int j = 0; j < 4; ++j) acc[j] = f32x2{0.f, 0.f};
  }
}

template <int N, int SLEEP, int BACK>
__device__ __forceinline__ void gather(u32x4 (&v)[N], __amdgpu_buffer_rsrc_t rws, unsigned (&off)[N], const unsigned (&so)[N], const Tag& tg) {
#pragma unroll
  for (int n = 0; n < N; ++n) v[n] = u32x4{0u, 0u, 0u, 0u};
#pragma unroll 1
  for (unsigned spins = 0; spins < SPIN_CAP; ++spins) {
    CBAR();
    u32x4 t[N];
#pragma unroll
    for (int n = 0; n < N; ++n) t[n] = __builtin_amdgcn_raw_buffer_load_b128(rws, off[n], so[n], 16);
    int left = 0;
#pragma unroll
    for (int n = 0; n < N; ++n) {
      const bool want = off[n] != OOB;
      if (want) v[n] = t[n];
      const bool miss = want && (tag_bad(t[n], tg) & 1023u) != 0;
      off[n] = miss ? off[n] : OOB;
      left += __builtin_popcountll(__builtin_amdgcn_ballot_w64(miss));
    }
    if (left == 0) break;
    if (SLEEP) __builtin_amdgcn_s_sleep(SLEEP);
    if (BACK) for (int i = left >> 4; i > 0; --i) __builtin_amdgcn_s_sleep(BACK);
  }
}

__device__ __forceinline__ unsigned touch(const char* p, unsigned off) {
  return *(const __attribute__((address_space(4))) unsigned*)(p + off);
}
typedef unsigned u32x8 __attribute__((ext_vector_type(8)));
__device__ __forceinline__ char* mkptr(unsigned lo, unsigned hi) { return (char*)(((unsigned long long)hi << 32) | lo); }

__global__ void __launch_bounds__(512, 2)
mha_fused(const char* __restrict__ x_, const char* __restrict__ kc_, const char* __restrict__ vc_,
          const char* __restrict__ wq_, const char* __restrict__ wo, char* __restrict__ out, char* __restrict__ ws_) {
  extern __shared__ char smem_raw[];
  LAS char* smem = (LAS char*)smem_raw;
  u32x8 ka; u32x2 kw, tm; unsigned long long pc; unsigned len;
  asm volatile("s_load_dwordx8 %0, %5, 0x0\n\ts_load_dwordx2 %1, %5, 0x30\n\ts_memrealtime %2\n\t"
               "s_getpc_b64 %3\n.Lmha_code_from:\n\ts_mov_b32 %4, .Lmha_code_to-.Lmha_code_from"
               : "=&s"(ka), "=&s"(kw), "=&s"(tm), "=&s"(pc), "=&s"(len) : "s"(__builtin_amdgcn_kernarg_segment_ptr()));
  SCHED();
  const int lane = threadIdx.x & 63;
  const int wave = __builtin_amdgcn_readfirstlane(threadIdx.x >> 6);
  const int blk = blockIdx.x;
  const unsigned pf = __builtin_amdgcn_raw_buffer_load_b32(mkrsrc((const void*)(pc & ~127ull), wave == 7 ? len : 0u), lane * 4096, (blk >> 3) * 128, 0);
  *lds_at<volatile unsigned>(smem, L_CNT + lane * 4) = max(lane - 17, 0) * SL;
  *lds_at<volatile unsigned>(smem, L_HDR + lane * 4) = 0u;
  SCHED();
  const int h = (blk & 7) * 4 + (blk >> 6), j = (blk >> 3) & 7;
  const bool strm = wave < 4;
  const int row0 = 512 * (j >> 2) + 128 * (wave & 3);
  const unsigned base = ((unsigned)h << 19) | ((unsigned)(j & 1) * 0x4040000u) | ((unsigned)(j & 2) << 23) | ((unsigned)(j & 4) << 14) | ((unsigned)wave << 12);
  u32x4 first[7];
  {
    unsigned vo = strm ? lane * 16 : (lane >> 2) * (HD * 2) + (lane & 3) * 16;
    const unsigned d1 = strm ? 0x400u : 16u * HD * 2, d4 = strm ? 0x8000u : 64u * HD * 2;
    unsigned so[7];
    so[0] = strm ? base : row0 * (HD * 2) + h * 256 + (j & 3) * 64;
    so[1] = so[0] + d1; so[2] = so[1] + d1; so[3] = so[2] + d1; so[4] = so[0] + d4; so[5] = so[4] + d1; so[6] = so[5] + d1;
    unsigned sz = strm ? KV_BYTES : DM * HD * 2;
    asm volatile("" : "+v"(vo));
    asm volatile("s_waitcnt lgkmcnt(0)\n\ts_barrier"
                 : "+s"(ka), "+s"(kw), "+s"(tm), "+s"(so[0]), "+s"(so[1]), "+s"(so[2]), "+s"(so[3]), "+s"(so[4]), "+s"(so[5]), "+s"(so[6]), "+s"(sz) :: "memory");
    const __amdgpu_buffer_rsrc_t rf = mkrsrc(mkptr(strm ? ka[2] : ka[6], strm ? ka[3] : ka[7]), sz);
#pragma unroll
    for (int i = 0; i < 7; ++i) first[i] = __builtin_amdgcn_raw_buffer_load_b128(rf, vo, so[i], 2);
    SCHED();
  }
  const char* const x = mkptr(ka[0], ka[1]); const char* const kc = mkptr(ka[2], ka[3]); const char* const vc = mkptr(ka[4], ka[5]);
  const char* const wq = mkptr(ka[6], ka[7]); char* const ws = mkptr(kw[0], kw[1]);
  const unsigned tstart = tm[0];

  static_assert(WIN >= 7, "first loads");
  if (strm) {
    Stream s;
    s.rk = mkrsrc(kc, KV_BYTES); s.rv = mkrsrc(vc, KV_BYTES); s.voff = lane * 16;
    Pace pc; pc.now = tstart; pc.lim = 0; pc.ext = 0; pc.due = 0; pc.mid = 0; pc.from = tstart + (unsigned)(TOFF + (WIN - 7) * TAU_T / 32);
    u32x4 ring[32];
    Run st;
    {
      u32x4 buf[64 + WIN];
#pragma unroll
      for (int n = 0; n < WIN; ++n) buf[n] = n < 7 ? first[n] : kv_load(s, n, base);
      SCHED();
#pragma unroll
      for (int n = 0; n < AHEAD; ++n) {
        asm volatile("" :: "v"(buf[n]));
        pace_gate(pc, n);
        buf[n + WIN] = kv_load(s, (n + WIN) & 31, base ^ unit_off((n + WIN) >> 5));
        SCHED();
      }
      lds_wait_ge(smem, C_Q, 1);
      att_batch<0>(s, pc, st, buf, base, 0, 0, smem, wave, lane);
      att_batch<1>(s, pc, st, buf, base, 0, 1, smem, wave, lane);
#pragma unroll
      for (int n = 0; n < WIN; ++n) ring[n] = buf[64 + n];
    }
    lds_wait_ge(smem, C_Q, 2);
    pc.lim = (int)(pc.now - pc.from) << 4;
    for (int u = 2; u < 15; ++u) {
      att_batch<2>(s, pc, st, ring, base ^ unit_off(u), base ^ unit_off(u + 1), u, smem, wave, lane);
    }
    s.ro = mkrsrc(wo, HD * DM * 2);
    s.wvoff = (lane >> 4) * (DM * 2) + (lane & 15) * 16;
    att_batch<3>(s, pc, st, ring, base ^ unit_off(15), (h * DH + 4 * wave) * (DM * 2) + j * 256, 15, smem, wave, lane);
    const int c = lane & 15, g = lane >> 4;
    Tag tg;
    make_tag(*lds_at<volatile unsigned>(smem, C_EP), tg);
#pragma unroll
    for (int i = 0; i < 8; ++i) *lds_at<volatile u32x4>(smem, L_WO + (16 * i + 4 * wave + g) * WO_ROW + c * 16) = ring[i];
    lds_inc(smem, C_WO, lane);
    lds_wait_ge<0>(smem, C_WO, 4);
    bf16x8 af[4][2];
#pragma unroll
    for (int k = 0; k < 4; ++k)
#pragma unroll
      for (int t = 0; t < 2; ++t) {
        const int ad = L_WO + (32 * k + 8 * g + (c >> 2)) * WO_ROW + wave * 64 + t * 32 + (c & 3) * 8;
        const s16x4 lo = __builtin_amdgcn_ds_read_tr16_b64_v4i16(lds_at<s16x4>(smem, ad));
        const s16x4 hi = __builtin_amdgcn_ds_read_tr16_b64_v4i16(lds_at<s16x4>(smem, ad + 4 * WO_ROW));
        af[k][t] = __builtin_bit_cast(bf16x8, __builtin_shufflevector(lo, hi, 0, 1, 2, 3, 4, 5, 6, 7));
        asm volatile("" : "+v"(af[k][t]));
      }
    const __amdgpu_buffer_rsrc_t rws = mkrsrc(ws, WS_SPAN);
    const unsigned pdst = WS_P + j * P_J + h * P_STRIDE + wave * 2048;
    u32x4 of[4];
#pragma unroll 1
    for (unsigned spins = 0; spins < SPIN_CAP; ++spins) {
      const unsigned f = *lds_at<volatile unsigned>(smem, C_O);
#pragma unroll
      for (int k = 0; k < 4; ++k) of[k] = *lds_at<volatile u32x4>(smem, L_O + c * O_ROW + k * 64 + g * 16);
      if ((unsigned)__builtin_amdgcn_readfirstlane((int)f) >= 4u) break;
    }
    f32x4 acc[2];
#pragma unroll
    for (int k = 0; k < 4; ++k)
#pragma unroll
      for (int t = 0; t < 2; ++t)
        acc[t] = __builtin_amdgcn_mfma_f32_16x16x32_bf16(af[k][t], __builtin_bit_cast(bf16x8, of[k]), k == 0 ? f32x4{0.f, 0.f, 0.f, 0.f} : acc[t], 0, 0, 0);
    __builtin_amdgcn_raw_buffer_store_b128(tag_pack(acc[0], tg), rws, lane * 16, pdst, 16);
    __builtin_amdgcn_raw_buffer_store_b128(tag_pack(acc[1], tg), rws, lane * 16 + 1024, pdst, 16);
    asm volatile(".Lmha_code_to:");
  } else {
    const int sw = wave - 4;
    const int c = lane & 15, g = lane >> 4, half = lane >> 5;
    const __amdgpu_buffer_rsrc_t rws = mkrsrc(ws, WS_SPAN);
    Tag tg;
    unsigned e1;
    {
      const __amdgpu_buffer_rsrc_t rq = mkrsrc(wq, DM * HD * 2), rx = mkrsrc(x, NB * DM * 2);
      u32x4 wb[8], xf[4];
#pragma unroll
      for (int i = 0; i < 7; ++i) wb[i] = first[i];
      wb[7] = __builtin_amdgcn_raw_buffer_load_b128(rq, (lane >> 2) * (HD * 2) + (lane & 3) * 16, (row0 + 16 * 7) * (HD * 2) + h * 256 + (j & 3) * 64, 2);
#pragma unroll
      for (int k = 0; k < 4; ++k) xf[k] = __builtin_amdgcn_raw_buffer_load_b128(rx, c * (DM * 2) + g * 16 + k * 64, row0 * 2, 0);
      const unsigned ep = __builtin_amdgcn_raw_buffer_load_b32(rws, 0, 0, 16);
      SCHED();
      if (sw == 3) {
        const unsigned b0 = base & ~0xffffu;
        asm volatile("" :: "s"(touch(vc, b0)), "s"(touch(vc, b0 ^ unit_off(1))), "s"(touch(kc, b0 ^ unit_off(1))));
      }
      SCHED();
#pragma unroll
      for (int i = 0; i < 8; ++i) *lds_at<volatile u32x4>(smem, L_WQ + sw * 8192 + i * 1024 + lane * 16) = wb[i];
      CBAR();
      f32x4 acc[2] = {f32x4{0.f, 0.f, 0.f, 0.f}, f32x4{0.f, 0.f, 0.f, 0.f}};
#pragma unroll
      for (int k = 0; k < 4; ++k)
#pragma unroll
        for (int t = 0; t < 2; ++t) {
          const int ad = L_WQ + sw * 8192 + (32 * k + 8 * g + (c >> 2)) * 64 + t * 32 + (c & 3) * 8;
          const s16x4 lo = __builtin_amdgcn_ds_read_tr16_b64_v4i16(lds_at<s16x4>(smem, ad));
          const s16x4 hi = __builtin_amdgcn_ds_read_tr16_b64_v4i16(lds_at<s16x4>(smem, ad + 256));
          const s16x8 fr = __builtin_shufflevector(lo, hi, 0, 1, 2, 3, 4, 5, 6, 7);
          acc[t] = __builtin_amdgcn_mfma_f32_16x16x32_bf16(__builtin_bit_cast(bf16x8, fr), __builtin_bit_cast(bf16x8, xf[k]), acc[t], 0, 0, 0);
        }
      *lds_at<volatile f32x4>(smem, L_QP + sw * 2048 + lane * 16) = acc[0];
      *lds_at<volatile f32x4>(smem, L_QP + sw * 2048 + 1024 + lane * 16) = acc[1];
      asm volatile("" :: "v"(pf));
      e1 = make_tag(ep, tg);
      *lds_at<volatile unsigned>(smem, C_EP) = ep;
      lds_inc(smem, C_QP, lane);
      if (sw < 2) {
        lds_wait_ge(smem, C_QP, 4);
        f32x4 sum = {0.f, 0.f, 0.f, 0.f};
#pragma unroll
        for (int w = 0; w < 4; ++w) sum += *lds_at<volatile f32x4>(smem, L_QP + w * 2048 + ((lane & 7) * 16 + 8 * sw + (lane >> 3)) * 16);
        __builtin_amdgcn_raw_buffer_store_b128(tag_pack(sum, tg), rws, lane * 16, WS_Q + (h * 8 + j) * Q_STRIDE + sw * 1024, 16);
      }
    }
    const unsigned grp = WS_PART + h * 8 * PART_SLOT;
    if (sw == 0) {
      const unsigned qo = (lane >> 3) * Q_STRIDE + (lane & 7) * 16, qs = WS_Q + h * 8 * Q_STRIDE + batch_of(0, j) * 128;
      {
        u32x4 v[1]; unsigned off[1] = {qo}; const unsigned so[1] = {qs};
        gather<1, QSLEEP, 0>(v, rws, off, so, tg);
        const f32x4 f = untag(v[0]);
        *lds_at<volatile unsigned>(smem, L_Q + (lane & 31) * 8 + half * 4) = pack_bf16(fold32(f[0], f[2]), fold32(f[1], f[3]));
        lds_inc(smem, C_Q, lane);
      }
      u32x4 v[3]; unsigned off[3], so[3];
#pragma unroll
      for (int n = 0; n < 3; ++n) { off[n] = qo; so[n] = qs + batch_of(n + 1, 0) * 128; }
      gather<3, QSLEEP, 0>(v, rws, off, so, tg);
#pragma unroll
      for (int n = 0; n < 3; ++n) {
        const f32x4 f = untag(v[n]);
        *lds_at<volatile unsigned>(smem, L_Q + (n + 1) * 256 + (lane & 31) * 8 + half * 4) = pack_bf16(fold32(f[0], f[2]), fold32(f[1], f[3]));
      }
      lds_inc(smem, C_Q, lane);
    } else if (sw == 1) {
      sleep_until(tstart + 900);
      asm volatile("" :: "s"(touch(ws, WS_L + 49152)), "s"(touch(ws, WS_L + L_Q_ + 49152)), "s"(touch(ws, WS_L + 2 * L_Q_ + 49152)), "s"(touch(ws, WS_L + 3 * L_Q_ + 49152)));
    } else if (sw == 2) {
      sleep_until(tstart + 800);
      const unsigned a = h * DH * DM * 2 + j * 256, p = WS_P + j * P_J;
      asm volatile("" :: "s"(touch(wo, a)), "s"(touch(wo, a + 65536)), "s"(touch(wo, a + 2 * 65536)), "s"(touch(wo, a + 3 * 65536)),
                   "s"(touch(ws, p - 128)), "s"(touch(ws, p + NH * P_STRIDE)),
                   "s"(touch(ws, WS_PART - 128)), "s"(touch(ws, WS_PART + 2 * PART_Q - 128)), "s"(touch(ws, WS_PART + 4 * PART_Q - 128)), "s"(touch(out, 0)));
    } else {
      sleep_until(tstart + 500);
      const unsigned b1 = (base & ~0xffffu) ^ unit_off(2);
      asm volatile("" :: "s"(touch(kc, b1)), "s"(touch(vc, b1)), "s"(touch(kc, b1 ^ unit_off(1))), "s"(touch(vc, b1 ^ unit_off(1))));
    }
    const int ch = lane & 7, g8 = lane >> 3;
    float ext = (float)((lane - 1) * SL), xprev = B0 - 2.5f * SL, skew = 0.f;
#pragma unroll 1
    for (int q = 0; q < 4; ++q) {
      if (sw >= 2 && q < 3) {
        if (q == 0) sleep_until(tstart + 1000);
        const unsigned b1 = (base & ~0xffffu) ^ unit_off(4 * q + 4);
        const char* const t = sw == 2 ? kc : vc;
        asm volatile("" :: "s"(touch(t, b1)), "s"(touch(t, b1 ^ unit_off(1))), "s"(touch(t, b1 ^ unit_off(2))), "s"(touch(t, b1 ^ unit_off(3))));
      }
      const unsigned src = grp + q * PART_Q;
      constexpr unsigned LSL = 128;
      const unsigned lq = WS_L + q * L_Q_;
      const unsigned lsrc = lq + h * 8 * LSL;
      lds_wait_flags<4>(smem, q, 2, lane);
      float en;
      {
        const u32x4 hd = *lds_at<volatile u32x4>(smem, L_HDR + (q * 4 + (g8 & 3)) * 16);
        float late = (float)hd[2];
        late += dpp<ROR8>(late); late = fold16(late, late);
        const float m = asf(hd[0]);
        const float M = max16(fmaxf(m, dpp<ROR8>(m)));
        const float e = __builtin_amdgcn_exp2f(m - M);
        float L = e * asf(hd[1]);
        L += dpp<ROR8>(L); L = fold16(L, L);
        en = e * __builtin_amdgcn_rcpf(L);
        const float lse = M + __builtin_amdgcn_logf(L), hi = asf(asu(lse) & 0xfffffc00u);
        if (sw == 0) __builtin_amdgcn_raw_buffer_store_b128(tag_pack(f32x4{hi, lse - hi, asf((unsigned)late << 10), asf(tstart << 10)}, tg), rws, lane < 8 ? lane * 16u : OOB, lsrc + j * LSL, 16);
        asm volatile("" : "+v"(en));
      }
      float wgt = 0.f;
      auto lse_look = [&]() {
        CBAR();
        const u32x4 lv = __builtin_amdgcn_raw_buffer_load_b128(rws, g8 * LSL + ch * 16, lsrc, 16);
        const f32x4 lf = untag(lv);
        auto a = __builtin_amdgcn_permlane32_swap(asu(lf[0]), asu(lf[0]), false, false);
        auto b = __builtin_amdgcn_permlane32_swap(asu(lf[1]), asu(lf[1]), false, false);
        const float df = (asf(a[1]) - asf(a[0])) + (asf(b[1]) - asf(b[0]));
        wgt = __builtin_amdgcn_rcpf(1.f + __builtin_amdgcn_exp2f(half ? -df : df));
        asm volatile("" : "+v"(wgt));
        return __builtin_amdgcn_ballot_w64((tag_bad(lv, tg) & 1023u) != 0u) == 0;
      };
      bool have = false;
      if (q < 3) lds_wait_flags<16>(smem, q, 3, lane);
      else {
        sleep_until((unsigned)__builtin_amdgcn_s_memrealtime() + LDLY);
        have = lse_look();
      }
      f32x4 o;
#pragma unroll 1
      for (unsigned spins = 0; spins < SPIN_CAP; ++spins) {
        const unsigned f = *lds_at<volatile unsigned>(smem, L_HDR + (q * 4 + (g8 & 3)) * 16 + 12);
        o = *lds_at<volatile f32x4>(smem, L_ACC + (q * 4 + (g8 & 3)) * 512 + sw * 128 + ch * 16);
        if (__builtin_amdgcn_ballot_w64(f != 1u) == 0) break;
      }
      o *= en;
#pragma unroll
      for (int i = 0; i < 4; ++i) { o[i] += dpp<ROR8>(o[i]); o[i] = fold16(o[i], o[i]); }
      __builtin_amdgcn_raw_buffer_store_b128(tag_pack(o, tg), rws, lane < 8 ? lane * 16u : OOB, src + j * PART_SLOT + sw * 128, 16);
      const unsigned t = (unsigned)__builtin_amdgcn_s_memrealtime();
      if (sw == 1 && q < 3) {
        sleep_until(t + MDLY);
        const u32x4 lv = __builtin_amdgcn_raw_buffer_load_b128(rws, (g8 * 32 + (ch & 3) * 8 + (((ch & 3) + (ch >> 2) * 3 + g8) & 7)) * LSL, lq, 16);
        const bool ok = (tag_bad(lv, tg) & 1023u) == 0u;
        float y = ok ? (float)((lv[2] >> 10) & 2047u) * 0.25f : MISS, g2 = ok ? y - (float)(lv[2] >> 21) * 0.25f : MISSG;
        g2 += dpp<QX1>(g2); g2 += dpp<QX2>(g2); g2 += dpp<HMIR>(g2); g2 += dpp<ROR8>(g2); g2 = fold16(g2, g2); g2 = fold32(g2, g2);
        if (q == 0) {
          float d = (float)((int)((tstart << 10) - (lv[3] & ~1023u)) >> 10);
          d += dpp<QX1>(d); d += dpp<QX2>(d); d += dpp<HMIR>(d);
          const float own = asf(__builtin_amdgcn_readlane(asu(d), (blk & 7) * 8));
          d += dpp<ROR8>(d); d = fold16(d, d); d = fold32(d, d);
          d = __builtin_amdgcn_ballot_w64(!ok) ? 0.f : d * (1.f / 64.f) - own * 0.125f;
          skew = fminf(fmaxf(d * SKG, -150.f), 150.f) * fminf((float)(lane + 1 - U0) * 0.1f, 1.f);
        }
        y += dpp<QX1>(y); y += dpp<QX2>(y); y += dpp<HMIR>(y);
        y += dpp<ROR8>(y);
        { const float ym = max32(max16(y)) * (1.f / 16.f); y = fold16(y, y); y = fold32(y, y); y = y * ((1.f - PAIR) / 64.f) + ym * PAIR; }
        const int u0 = 4 * q + U0;
        const float em = 0.5f * (asf(__builtin_amdgcn_readlane(asu(ext), 4 * q + 2)) + asf(__builtin_amdgcn_readlane(asu(ext), 4 * q + 3)));
        const float e0 = asf(__builtin_amdgcn_readlane(asu(ext), u0 - 1)), e15 = asf(__builtin_amdgcn_readlane(asu(ext), 15));
        const float xq = em + y, rem = (float)(16 - u0);
        g2 = (g2 * (1.f / 64.f) + em - asf(__builtin_amdgcn_readlane(asu(ext), 4 * q + 1))) * (1.f / 1.5f);
        float grow = fminf(fminf((xq - xprev) * 0.25f, (xq + 2.f * SL) / (float)(4 * q + 4)) * (q == 0 ? GAIN0 : GAIN), g2);
        grow = fmaxf(grow, fminf(y - 60.f, 0.f) * 0.2f + 4.f);
        const bool act = y > (q == 0 && g2 < SL + 15.f ? D0 : DEAD);
        const float keep = fminf(KEEP + (e15 - e0) / rem * KSL, KMAX);
        float e = act ? fmaxf(fminf(xq + grow * (12.5f - 4 * q), 1500.f), e0 + rem * (SL * KEEP))
                  : y > (q == 0 ? CLEAN0 : CLEAN) ? fminf(e15, fmaxf(e0 + (e15 - e0) * keep, fminf(e0 + rem * SL, em + 1.5f * y + fmaxf(g2, 0.f) * (12.5f - 4 * q)))) : e0 + (e15 - e0) * keep;
        xprev = xq;
        const float front = act ? fmaxf(e - e0 - fmaxf(grow, 0.f) * rem, 0.f) * BF : 0.f, k = (float)(lane + 1 - u0);
        if (lane >= u0) ext = e0 + (e - e0 - front) * k / rem + front * fminf(k * 0.5f, 1.f);
        if (lane < 16 && lane >= u0) *lds_at<volatile int>(smem, C_EXT + lane * 4) = (int)(ext - skew);
      }
      if (q < 3) sleep_until(t + EDLY);
      if (!have) {
#pragma unroll 1
        for (unsigned spins = 0; spins < SPIN_CAP; ++spins) {
          if (lse_look()) break;
          if (q < 3) __builtin_amdgcn_s_sleep(ERETRY); else __builtin_amdgcn_s_sleep(LRETRY);
        }
      }
      wait_until(t + GDLY, t);
      u32x4 v;
#pragma unroll 1
      for (unsigned spins = 0; spins < SPIN_CAP; ++spins) {
        CBAR();
        v = __builtin_amdgcn_raw_buffer_load_b128(rws, g8 * PART_SLOT + ch * 16, src + sw * 128, 16);
        const bool okk = __builtin_amdgcn_ballot_w64((tag_bad(v, tg) & 1023u) != 0u) == 0;
        if (okk) break;
        if (q < 3) __builtin_amdgcn_s_sleep(ERETRY);
      }
      const f32x4 w = untag(v) * wgt;
      *lds_at<volatile unsigned>(smem, L_O + batch_of(q, g8) * O_ROW + sw * 64 + ch * 8 + half * 4) = pack_bf16(fold32(w[0], w[2]), fold32(w[1], w[3]));
    }
    lds_inc(smem, C_O, lane);
    lds_wait_ge<0>(smem, C_O, 4);
    { const unsigned t_ = (unsigned)__builtin_amdgcn_s_memrealtime(); wait_until(t_ + FDLY, t_); }
    u32x4 v[2]; unsigned off[2], so[2];
#pragma unroll
    for (int n = 0; n < 2; ++n) { off[n] = (lane >> 2) * P_STRIDE + (lane & 3) * 16; so[n] = WS_P + j * P_J + 16 * n * P_STRIDE + h * 256 + sw * 64; }
    gather<2, FSLEEP, 0>(v, rws, off, so, tg);
    const f32x4 sum = untag(v[0]) + untag(v[1]);
    float y = fold16(fold32(sum[0], sum[1]), fold32(sum[2], sum[3]));
    y += dpp<ROR4>(y); y += dpp<ROR8>(y);
    auto sw32 = __builtin_amdgcn_permlane32_swap(asu(y), asu(y), false, false);
    if (lane < 32 && c < 4) *(unsigned*)(out + (4 * sw + c) * (DM * 2) + (j * 128 + 4 * h + 2 * g) * 2) = pack_bf16(asf(sw32[0]), asf(sw32[1]));
    if (blk == 0 && sw == 0 && lane == 0) __builtin_amdgcn_raw_buffer_store_b32(e1, rws, 0, 0, 16);
  }
}

extern "C" void kernel_launch(void* const* d_in, const int* in_sizes, int n_in, void* d_out, int out_size, void* d_ws, size_t ws_size, hipStream_t stream) {
  (void)hipFuncSetAttribute((const void*)mha_fused, hipFuncAttributeMaxDynamicSharedMemorySize, LDS_BYTES);
  mha_fused<<<256, 512, LDS_BYTES, stream>>>((const char*)d_in[0], (const char*)d_in[1], (const char*)d_in[2],
                                             (const char*)d_in[3], (const char*)d_in[4], (char*)d_out, (char*)d_ws);
}
```
